```python
import torch

def _softmax(x, axis=-1):
    return torch.softmax(x, dim=axis)
def _gelu(x):
    return torch.nn.functional.gelu(x, approximate="tanh")
def _silu(x):
    return torch.nn.functional.silu(x)
def _sigmoid(x):
    return torch.sigmoid(x)
def _layernorm(x, axis=-1, eps=1e-5):
    m = x.mean(dim=axis, keepdim=True)
    v = x.var(dim=axis, keepdim=True, unbiased=False)
    return (x - m) / torch.sqrt(v + eps)
def _scatter_add(tgt, idx, src, axis=0):
    return tgt.clone().index_add_(axis, idx, src)
def _segment_sum(src, seg, n):
    out = torch.zeros((n,) + src.shape[1:], dtype=src.dtype)
    return out.index_add_(0, seg, src)
def _pad(x, pw):
    flat = tuple(v for pair in reversed(pw) for v in pair)
    return torch.nn.functional.pad(x, flat)

kernel_name = 'gated_mix__sliding_block_cached__sliding_block_cached__decode'

def setup_inputs():

    _w = (lambda a: (lambda t: t.to(torch.bfloat16) if t.is_floating_point() else t)(torch.as_tensor(a)))
    def _A_setup():
        B, Sq, D = 1, 1, 512
        Hq, Hkv, Sk, Dh = 32, 8, 32768, 128
        assert Hq % Hkv == 0, (Hq, Hkv)
        return {
            "x": _w((lambda *s: torch.randn(*s, dtype=torch.bfloat16) * 0.05)(B, Sq, D)),
            "k_cache": _w((lambda *s: torch.randn(*s, dtype=torch.bfloat16) * 6.4)(B, Hkv, Sk, Dh)),
            "v_cache": _w((lambda *s: torch.randn(*s, dtype=torch.bfloat16) * 0.05)(B, Hkv, Sk, Dh)),
            "wq": _w((lambda *s: torch.randn(*s, dtype=torch.bfloat16) * 6.4)(D, Hq * Dh) * (D ** -0.5)),
            "wo": _w((lambda *s: torch.randn(*s, dtype=torch.bfloat16) * 0.05)(Hq * Dh, D) * ((Hq * Dh) ** -0.5)),
        }
    _A_in = _A_setup()

    def _B_setup():
        B, Sq, D = 1, 1, 512
        Hq, Hkv, Sk, Dh = 32, 8, 32768, 128
        assert Hq % Hkv == 0, (Hq, Hkv)
        return {
            "x": _w((lambda *s: torch.randn(*s, dtype=torch.bfloat16) * 0.05)(B, Sq, D)),
            "k_cache": _w((lambda *s: torch.randn(*s, dtype=torch.bfloat16) * 6.4)(B, Hkv, Sk, Dh)),
            "v_cache": _w((lambda *s: torch.randn(*s, dtype=torch.bfloat16) * 0.05)(B, Hkv, Sk, Dh)),
            "wq": _w((lambda *s: torch.randn(*s, dtype=torch.bfloat16) * 6.4)(D, Hq * Dh) * (D ** -0.5)),
            "wo": _w((lambda *s: torch.randn(*s, dtype=torch.bfloat16) * 0.05)(Hq * Dh, D) * ((Hq * Dh) ** -0.5)),
        }
    _B_in = _B_setup()

    return {
        "A_x": _A_in["x"],
        "A_k_cache": _A_in["k_cache"],
        "A_v_cache": _A_in["v_cache"],
        "A_wq": _A_in["wq"],
        "A_wo": _A_in["wo"],
        "B_x": _B_in["x"],
        "B_k_cache": _B_in["k_cache"],
        "B_v_cache": _B_in["v_cache"],
        "B_wq": _B_in["wq"],
        "B_wo": _B_in["wo"],
        "gate_w": _w((lambda *s: torch.randn(*s, dtype=torch.bfloat16) * 0.05)(512) * 16.0),
    }

def reference(A_x, A_k_cache, A_v_cache, A_wq, A_wo, B_x, B_k_cache, B_v_cache, B_wq, B_wo, gate_w):
    def _A_ref(x, k_cache, v_cache, wq, wo):
        B, Sq, D = x.shape
        Hq, Dh = 32, 128
        Hkv, Sk = k_cache.shape[1], k_cache.shape[2]
        W = min(4096, max(256, Sk // 4))
        rep = Hq // Hkv
        q = (lambda x, *d: x.permute(*d))((x @ wq).reshape(B, Sq, Hq, Dh), 0, 2, 1, 3)
        k = (lambda x, n, axis: torch.repeat_interleave(x, n, dim=axis))(k_cache[:, :, Sk - W:, :], rep, axis=1)
        v = (lambda x, n, axis: torch.repeat_interleave(x, n, dim=axis))(v_cache[:, :, Sk - W:, :], rep, axis=1)
        scores = torch.einsum("bhqd,bhkd->bhqk", q, k) * (Dh ** -0.5)
        p = _softmax(scores, axis=-1)
        o = torch.einsum("bhqk,bhkd->bhqd", p, v)
        return (lambda x, *d: x.permute(*d))(o, 0, 2, 1, 3).reshape(B, Sq, Hq * Dh) @ wo

    def _B_ref(x, k_cache, v_cache, wq, wo):
        B, Sq, D = x.shape
        Hq, Dh = 32, 128
        Hkv, Sk = k_cache.shape[1], k_cache.shape[2]
        W = min(4096, max(256, Sk // 4))
        rep = Hq // Hkv
        q = (lambda x, *d: x.permute(*d))((x @ wq).reshape(B, Sq, Hq, Dh), 0, 2, 1, 3)
        k = (lambda x, n, axis: torch.repeat_interleave(x, n, dim=axis))(k_cache[:, :, Sk - W:, :], rep, axis=1)
        v = (lambda x, n, axis: torch.repeat_interleave(x, n, dim=axis))(v_cache[:, :, Sk - W:, :], rep, axis=1)
        scores = torch.einsum("bhqd,bhkd->bhqk", q, k) * (Dh ** -0.5)
        p = _softmax(scores, axis=-1)
        o = torch.einsum("bhqk,bhkd->bhqd", p, v)
        return (lambda x, *d: x.permute(*d))(o, 0, 2, 1, 3).reshape(B, Sq, Hq * Dh) @ wo

    x = A_x
    g = _sigmoid((x * gate_w).sum(-1, keepdims=True))
    a = _A_ref(x, A_k_cache, A_v_cache, A_wq, A_wo)
    b = _B_ref(x, B_k_cache, B_v_cache, B_wq, B_wo)
    return g * a + (1 - g) * b
```

```cpp
#include <hip/hip_runtime.h>
#include <hip/hip_bf16.h>
#include <stdint.h>

typedef unsigned short u16;
__device__ __forceinline__ float bf2f(u16 b){ return __uint_as_float(((unsigned)b)<<16); }
__device__ __forceinline__ float rbf(float f){
  unsigned u = __float_as_uint(f); u += 0x7FFFu + ((u>>16)&1u); return __uint_as_float(u & 0xFFFF0000u); }
__device__ __forceinline__ u16 f2bf(float f){ unsigned u = __float_as_uint(f); u += 0x7FFFu + ((u>>16)&1u); return (u16)(u>>16); }

constexpr int D=512, HQ=32, HKV=8, SK=32768, DH=128, W=4096, NS=16;

__global__ void k_q(const u16* x, const u16* wqA, const u16* wqB, float* q){
  int br = blockIdx.y; const u16* wq = br? wqB: wqA;
  int c = blockIdx.x*256 + threadIdx.x;
  float acc=0.f;
  for(int i=0;i<D;i++) acc += bf2f(x[i])*bf2f(wq[(size_t)i*4096 + c]);
  q[br*4096+c] = rbf(acc);
}
__global__ void k_attn(const float* q, const u16* kA, const u16* vA, const u16* kB, const u16* vB, float* po, float* pml){
  int s = blockIdx.x, kvh = blockIdx.y, br = blockIdx.z;
  const u16* K = (br? kB:kA) + ((size_t)kvh*SK + (SK-W) + s*256)*DH;
  const u16* V = (br? vB:vA) + ((size_t)kvh*SK + (SK-W) + s*256)*DH;
  __shared__ float qs[4][128]; __shared__ float p[4][256]; __shared__ float red[4][256];
  int t = threadIdx.x;
  for(int i=t;i<512;i+=256) qs[i/128][i%128] = q[br*4096 + kvh*512 + i];
  __syncthreads();
  float sc[4]={0,0,0,0};
  for(int d=0;d<128;d++){ float kv = bf2f(K[(size_t)t*DH+d]); for(int h=0;h<4;h++) sc[h]+= qs[h][d]*kv; }
  for(int h=0;h<4;h++){ sc[h] = rbf(rbf(sc[h])*0.08838834764831845f); red[h][t]=sc[h]; }
  __syncthreads();
  float m[4]; for(int h=0;h<4;h++){ float mm=-INFINITY; for(int i=0;i<256;i++) mm=fmaxf(mm,red[h][i]); m[h]=mm; }
  __syncthreads();
  for(int h=0;h<4;h++){ p[h][t]=__expf(sc[h]-m[h]); }
  __syncthreads();
  int h=t/64, d0=(t%64)*2; float o0=0,o1=0,l=0;
  for(int k=0;k<256;k++){ float pp=p[h][k]; o0+=pp*bf2f(V[(size_t)k*DH+d0]); o1+=pp*bf2f(V[(size_t)k*DH+d0+1]); l+=pp; }
  int hq = kvh*4+h;
  po[(((size_t)br*32+hq)*NS+s)*128+d0]=o0; po[(((size_t)br*32+hq)*NS+s)*128+d0+1]=o1;
  if((t%64)==0){ pml[(((size_t)br*32+hq)*NS+s)*2]=m[h]; pml[(((size_t)br*32+hq)*NS+s)*2+1]=l; }
}
__global__ void k_comb(const float* po, const float* pml, float* o){
  int hq=blockIdx.x, br=blockIdx.y, d=threadIdx.x;
  float M=-INFINITY; for(int s=0;s<NS;s++) M=fmaxf(M,pml[(((size_t)br*32+hq)*NS+s)*2]);
  float L=0,a=0; for(int s=0;s<NS;s++){ float w=__expf(pml[(((size_t)br*32+hq)*NS+s)*2]-M); L+=w*pml[(((size_t)br*32+hq)*NS+s)*2+1]; a+=w*po[(((size_t)br*32+hq)*NS+s)*128+d]; }
  o[br*4096+hq*128+d]=rbf(a/L);
}
__global__ void k_out(const u16* x, const u16* gw, const float* o, const u16* woA, const u16* woB, u16* out){
  int c = blockIdx.x*64+ (threadIdx.x&63); int part = threadIdx.x>>6;
  __shared__ float sa[16][64], sb[16][64];
  float a=0,b=0;
  for(int r=part*256;r<part*256+256;r++){ a+=o[r]*bf2f(woA[(size_t)r*512+c]); b+=o[4096+r]*bf2f(woB[(size_t)r*512+c]); }
  sa[part][threadIdx.x&63]=a; sb[part][threadIdx.x&63]=b;
  __syncthreads();
  if(part==0){
    float A=0,B=0; for(int i=0;i<16;i++){A+=sa[i][threadIdx.x];B+=sb[i][threadIdx.x];}
    float gs=0; for(int i=0;i<512;i++) gs+=rbf(bf2f(x[i])*bf2f(gw[i]));
    gs=rbf(gs); float g=rbf(1.f/(1.f+__expf(-gs)));
    A=rbf(A);B=rbf(B);
    float r=rbf(rbf(g*A)+rbf(rbf(1.f-g)*B));
    out[c]=f2bf(r);
  }
}
extern "C" void kernel_launch(void* const* d_in, const int* in_sizes, int n_in, void* d_out, int out_size, void* d_ws, size_t ws_size, hipStream_t stream){
  const u16* x=(const u16*)d_in[0]; const u16* kA=(const u16*)d_in[1]; const u16* vA=(const u16*)d_in[2]; const u16* wqA=(const u16*)d_in[3]; const u16* woA=(const u16*)d_in[4];
  const u16* kB=(const u16*)d_in[6]; const u16* vB=(const u16*)d_in[7]; const u16* wqB=(const u16*)d_in[8]; const u16* woB=(const u16*)d_in[9]; const u16* gw=(const u16*)d_in[10];
  float* q=(float*)d_ws; float* po=q+8192; float* pml=po+2*32*NS*128; float* o=pml+2*32*NS*2;
  k_q<<<dim3(16,2),256,0,stream>>>(x,wqA,wqB,q);
  k_attn<<<dim3(NS,8,2),256,0,stream>>>(q,kA,vA,kB,vB,po,pml);
  k_comb<<<dim3(32,2),128,0,stream>>>(po,pml,o);
  k_out<<<8,1024,0,stream>>>(x,gw,o,woA,woB,(u16*)d_out);
}
```

```cpp
#include <hip/hip_runtime.h>
#include <stdint.h>

typedef unsigned v4u __attribute__((ext_vector_type(4)));
typedef unsigned v2u __attribute__((ext_vector_type(2)));
typedef float f32x4 __attribute__((ext_vector_type(4)));
typedef float f32x2 __attribute__((ext_vector_type(2)));
typedef short bf16x8 __attribute__((ext_vector_type(8)));
typedef __bf16 bf16x2 __attribute__((ext_vector_type(2)));
typedef unsigned long long u64;
#define GAS __attribute__((address_space(1)))
#define RLX_AGENT __ATOMIC_RELAXED, __HIP_MEMORY_SCOPE_AGENT
#define SB() __builtin_amdgcn_sched_barrier(0)

constexpr int SK = 32768, WIN = 4096, DH = 128;
constexpr int AUX_NT = 2, AUX_SC1 = 16, RS_FLAGS = 0x00020000;
constexpr int WS_EPOCH = 0;
constexpr int WS_Q = 4096;
constexpr int WS_ST = WS_Q + 16 * 16 * 128;
constexpr int WS_O = WS_ST + 16 * 16 * 4 * 16;
constexpr int WS_F = WS_O + 16 * 16 * 4 * 128 * 8;
constexpr int WS_END = WS_F + 64 * 512 * 8;
constexpr int WS_STAMP = 4 << 20;
constexpr unsigned SPIN_CAP = 1u << 14;
constexpr int LDS_BYTES = 96 * 1024;

struct Params {
  const unsigned short *x, *kA, *vA, *wqA, *woA, *kB, *vB, *wqB, *woB, *gw;
  unsigned short* out;
  unsigned char* ws;
};

__device__ __forceinline__ float dot2(unsigned a, unsigned b, float c) {
  return __builtin_amdgcn_fdot2_f32_bf16(__builtin_bit_cast(bf16x2, a), __builtin_bit_cast(bf16x2, b), c, false);
}
__device__ __forceinline__ unsigned cvtpk(float lo, float hi) {
  bf16x2 r = {(__bf16)lo, (__bf16)hi};
  return __builtin_bit_cast(unsigned, r);
}
__device__ __forceinline__ float bflo(unsigned u) { return __uint_as_float(u << 16); }
__device__ __forceinline__ float bfhi(unsigned u) { return __uint_as_float(u & 0xffff0000u); }
__device__ __forceinline__ float rbf(float x) { return bflo(cvtpk(x, x) & 0xffffu); }
template <int CTRL> __device__ __forceinline__ float dpp(float x) {
  return __int_as_float(__builtin_amdgcn_mov_dpp(__float_as_int(x), CTRL, 0xf, 0xf, true));
}
constexpr int XOR1 = 0xB1, XOR2 = 0x4E, HMIR = 0x141, MIR = 0x140, ROR8 = 0x128, SHL4 = 0x104, SHR4 = 0x114;
__device__ __forceinline__ float xor4(float x) {
  int a = __builtin_amdgcn_update_dpp(0, __float_as_int(x), SHL4, 0xf, 0x5, false);
  a = __builtin_amdgcn_update_dpp(a, __float_as_int(x), SHR4, 0xf, 0xa, false);
  return __int_as_float(a);
}
__device__ __forceinline__ float tr32(float a, float b) {
  auto r = __builtin_amdgcn_permlane32_swap(__float_as_uint(a), __float_as_uint(b), false, false);
  return __uint_as_float(r[0]) + __uint_as_float(r[1]);
}
__device__ __forceinline__ float tr16(float a, float b) {
  auto r = __builtin_amdgcn_permlane16_swap(__float_as_uint(a), __float_as_uint(b), false, false);
  return __uint_as_float(r[0]) + __uint_as_float(r[1]);
}
__device__ __forceinline__ float max32(float a) {
  auto r = __builtin_amdgcn_permlane32_swap(__float_as_uint(a), __float_as_uint(a), false, false);
  return fmaxf(__uint_as_float(r[0]), __uint_as_float(r[1]));
}
__device__ __forceinline__ float max16(float a) {
  auto r = __builtin_amdgcn_permlane16_swap(__float_as_uint(a), __float_as_uint(a), false, false);
  return fmaxf(__uint_as_float(r[0]), __uint_as_float(r[1]));
}
__device__ __forceinline__ float sum32(float a) { return tr32(a, a); }
__device__ __forceinline__ float sum16(float a) { return tr16(a, a); }
__device__ __forceinline__ f32x4 mfma(v4u a, v4u b, f32x4 c) {
  return __builtin_amdgcn_mfma_f32_16x16x32_bf16(__builtin_bit_cast(bf16x8, a), __builtin_bit_cast(bf16x8, b), c, 0, 0, 0);
}
#define STAMP(i)

__device__ __forceinline__ unsigned g_lo(float v, unsigned tl) { return ((__float_as_uint(v) + 0x80u) & 0xffffff00u) | tl; }
__device__ __forceinline__ float g_val(unsigned lo) { return __uint_as_float(lo & 0xffffff00u); }

__global__ void __launch_bounds__(256, 1) fused_decode(Params p) {
  extern __shared__ __attribute__((aligned(16))) unsigned char smem[];
  float* lds = (float*)smem;
  float* L_qred = lds;
  float* L_max = lds + 128;
  float* L_sum = lds + 144;
  float* L_o = lds + 256;
  unsigned short* L_ob = (unsigned short*)(lds + 256 + 2048);
  float* L_f = lds + 256 + 2048 + 64;
  STAMP(0);
  const int b = blockIdx.x, tid = threadIdx.x, lane = tid & 63;
  const int w = __builtin_amdgcn_readfirstlane(tid >> 6);
  const int kvh = b & 7, br = b >> 7, s = (b >> 3) & 15, grp = br * 8 + kvh;
  const unsigned short* kp = (br ? p.kB : p.kA) + ((size_t)kvh * SK + (SK - WIN) + s * 256) * DH;
  const unsigned short* vp = (br ? p.vB : p.vA) + ((size_t)kvh * SK + (SK - WIN) + s * 256) * DH;
  const unsigned short* wqp = (br ? p.wqB : p.wqA) + kvh * 512 + s * 32;
  const unsigned short* wop = (br ? p.woB : p.woA) + (size_t)(kvh * 512 + (s >> 2) * 128) * 512 + (s & 3) * 128;
  const __amdgpu_buffer_rsrc_t rK = __builtin_amdgcn_make_buffer_rsrc((void*)kp, 0, 65536, RS_FLAGS);
  const __amdgpu_buffer_rsrc_t rV = __builtin_amdgcn_make_buffer_rsrc((void*)vp, 0, 65536, RS_FLAGS);
  const __amdgpu_buffer_rsrc_t rQ = __builtin_amdgcn_make_buffer_rsrc((void*)wqp, 0, 511 * 8192 + 64, RS_FLAGS);
  const __amdgpu_buffer_rsrc_t rO = __builtin_amdgcn_make_buffer_rsrc((void*)wop, 0, 127 * 1024 + 256, RS_FLAGS);
  const __amdgpu_buffer_rsrc_t rW = __builtin_amdgcn_make_buffer_rsrc((void*)p.ws, 0, WS_END, RS_FLAGS);
  const __amdgpu_buffer_rsrc_t rX = __builtin_amdgcn_make_buffer_rsrc((void*)p.x, 0, 1024, RS_FLAGS);
  const __amdgpu_buffer_rsrc_t rG = __builtin_amdgcn_make_buffer_rsrc((void*)p.gw, 0, 1024, RS_FLAGS);

  const v2u ep = __builtin_amdgcn_raw_buffer_load_b64(rW, WS_EPOCH, 0, AUX_SC1);
  const v4u xf = __builtin_amdgcn_raw_buffer_load_b128(rX, (128 * w + 8 * (lane >> 2)) * 2, 0, 0);
  const v4u xa = __builtin_amdgcn_raw_buffer_load_b128(rX, lane * 16, 0, 0);
  const v4u ga = __builtin_amdgcn_raw_buffer_load_b128(rG, lane * 16, 0, 0);
  SB();
  v4u wq[8];
  {
    const int vo = (128 * w + 8 * (lane >> 2)) * 8192 + (lane & 3) * 16;
#pragma unroll
    for (int i = 0; i < 8; ++i) wq[i] = __builtin_amdgcn_raw_buffer_load_b128(rQ, vo, i * 8192, AUX_NT);
  }
  SB();
  v4u kf[16], vf[16], wo[8];
  const int kvo = w * 16384 + ((lane & 15) >> 2) * 256 + (lane >> 4) * 64 + (lane & 3) * 16;
  const int vvo = w * 16384 + lane * 16;
  constexpr int K1 = 4;
#pragma unroll
  for (int i = 0; i < K1; ++i) kf[i] = __builtin_amdgcn_raw_buffer_load_b128(rK, kvo, i * 1024, AUX_NT);
  SB();
  const u64 e64 = (((u64)ep.y << 32) | ep.x) + 1ull;
  const unsigned t32 = (unsigned)e64;
  const unsigned tlo = (unsigned)e64 & 0xffu, thi = (unsigned)(e64 >> 8);
  float gate;
  {
    float gs = 0.f;
#pragma unroll
    for (int j = 0; j < 4; ++j) {
      const unsigned pr = cvtpk(bflo(xa[j]) * bflo(ga[j]), bfhi(xa[j]) * bfhi(ga[j]));
      gs += bflo(pr) + bfhi(pr);
    }
    gs += dpp<XOR1>(gs); gs += dpp<XOR2>(gs); gs += dpp<HMIR>(gs); gs += dpp<MIR>(gs);
    gs = sum16(gs); gs = sum32(gs);
    gs = rbf(gs);
    gate = rbf(1.f / (1.f + __expf(-gs)));
  }
  {
    float acc[8];
#pragma unroll
    for (int j = 0; j < 8; ++j) acc[j] = 0.f;
#pragma unroll
    for (int k = 0; k < 4; ++k)
#pragma unroll
      for (int j = 0; j < 4; ++j) {
        const unsigned lo = __builtin_amdgcn_perm(wq[2 * k + 1][j], wq[2 * k][j], 0x05040100u);
        const unsigned hi = __builtin_amdgcn_perm(wq[2 * k + 1][j], wq[2 * k][j], 0x07060302u);
        acc[2 * j] = dot2(lo, xf[k], acc[2 * j]);
        acc[2 * j + 1] = dot2(hi, xf[k], acc[2 * j + 1]);
      }
    float a4[4], a2[2];
#pragma unroll
    for (int j = 0; j < 4; ++j) a4[j] = tr32(acc[j], acc[j + 4]);
#pragma unroll
    for (int j = 0; j < 2; ++j) a2[j] = tr16(a4[j], a4[j + 2]);
    const float t0 = a2[0] + dpp<ROR8>(a2[0]), t1 = a2[1] + dpp<ROR8>(a2[1]);
    float a1 = (lane & 8) ? t1 : t0;
    a1 += xor4(a1);
    const int col = 8 * (lane & 3) + ((lane >> 3) & 1) + 2 * ((lane >> 4) & 1) + 4 * (lane >> 5);
    if (!(lane & 4)) L_qred[w * 32 + col] = a1;
  }
  STAMP(1);
  __syncthreads();
  if (w == 0 && lane < 16) {
    float q0 = 0.f, q1 = 0.f;
#pragma unroll
    for (int ww = 0; ww < 4; ++ww) { const f32x2 t = *(const f32x2*)(L_qred + ww * 32 + 2 * lane); q0 += t.x; q1 += t.y; }
    v2u g; g.x = cvtpk(q0, q1); g.y = t32;
    __builtin_amdgcn_raw_buffer_store_b64(g, rW, WS_Q + (grp * 16 + s) * 128 + lane * 8, 0, AUX_SC1);
  }
  SB();
  STAMP(2);
#pragma unroll
  for (int i = K1; i < 16; ++i) kf[i] = __builtin_amdgcn_raw_buffer_load_b128(rK, kvo, i * 1024, AUX_NT);
  SB();
  constexpr int V1 = 4;
#pragma unroll
  for (int i = 0; i < V1; ++i) vf[i] = __builtin_amdgcn_raw_buffer_load_b128(rV, vvo, i * 1024, AUX_NT);
  SB();
  const int c16 = lane & 15, g4 = lane >> 4;
  const int hc = (c16 >> 3) + 2 * ((c16 >> 2) & 1);
  const int qoff = WS_Q + (grp * 16 + 4 * hc + g4) * 128 + (c16 & 3) * 32;
  v4u qa = __builtin_amdgcn_raw_buffer_load_b128(rW, qoff, 0, AUX_SC1);
  v4u qb = __builtin_amdgcn_raw_buffer_load_b128(rW, qoff + 16, 0, AUX_SC1);
  SB();
#pragma unroll
  for (int i = V1; i < 16; ++i) vf[i] = __builtin_amdgcn_raw_buffer_load_b128(rV, vvo, i * 1024, AUX_NT);
  SB();
  STAMP(3);
  if (!__all((qa.y == t32) & (qa.w == t32) & (qb.y == t32) & (qb.w == t32))) {
    for (unsigned spins = 0; spins < SPIN_CAP; ++spins) {
      qa = __builtin_amdgcn_raw_buffer_load_b128(rW, qoff, 0, AUX_SC1);
      qb = __builtin_amdgcn_raw_buffer_load_b128(rW, qoff + 16, 0, AUX_SC1);
      if (__all((qa.y == t32) & (qa.w == t32) & (qb.y == t32) & (qb.w == t32))) break;
      __builtin_amdgcn_s_sleep(1);
    }
  }
  STAMP(4);
  const v4u qf = {qa.x, qa.z, qb.x, qb.z};
  SB();
  {
    const int vo = (32 * w + 8 * (lane >> 4)) * 1024 + (lane & 15) * 16;
#pragma unroll
    for (int i = 0; i < 8; ++i) wo[i] = __builtin_amdgcn_raw_buffer_load_b128(rO, vo, i * 1024, AUX_NT);
  }
  SB();
  float sc[16];
  const bool b0 = lane & 1, b1 = lane & 2;
#pragma unroll
  for (int i = 0; i < 16; ++i) {
    const f32x4 d = mfma(kf[i], qf, f32x4{0.f, 0.f, 0.f, 0.f});
    const float t0 = b0 ? d[1] : d[0], t1 = b0 ? d[3] : d[2];
    float v = b1 ? t1 : t0;
    v += dpp<XOR1>(v); v += dpp<XOR2>(v);
    sc[i] = v;
  }
#pragma unroll
  for (int i = 0; i < 16; i += 2) {
    const unsigned r1 = cvtpk(sc[i], sc[i + 1]);
    const unsigned r2 = cvtpk(bflo(r1) * 0.08838834764831845f, bfhi(r1) * 0.08838834764831845f);
    sc[i] = bflo(r2); sc[i + 1] = bfhi(r2);
  }
  float m = sc[0];
#pragma unroll
  for (int i = 1; i < 16; ++i) m = fmaxf(m, sc[i]);
  m = max16(m); m = max32(m);
  if (lane < 16 && !(lane & 3)) L_max[hc * 4 + w] = m;
  __syncthreads();
  {
    const f32x4 mm = *(const f32x4*)(L_max + hc * 4);
    m = fmaxf(fmaxf(mm[0], mm[1]), fmaxf(mm[2], mm[3]));
  }
  float l = 0.f;
  unsigned pk[8];
  constexpr float LOG2E = 1.4426950408889634f;
  const float mb = m * LOG2E;
#pragma unroll
  for (int i = 0; i < 16; i += 2) {
    const float p0 = __builtin_amdgcn_exp2f(sc[i] * LOG2E - mb), p1 = __builtin_amdgcn_exp2f(sc[i + 1] * LOG2E - mb);
    l += p0 + p1;
    pk[i >> 1] = cvtpk(p0, p1);
  }
  l = sum16(l); l = sum32(l);
  if (lane < 16 && !(lane & 3)) L_sum[hc * 4 + w] = l;
  STAMP(5);
  unsigned selE[4], selO[4];
  {
    const int jp = lane & 7;
#pragma unroll
    for (int r = 0; r < 4; ++r) {
      const bool on = (jp >> 1) == r;
      const unsigned e = (jp & 1) ? 0x01000c0cu : 0x0c0c0100u;
      const unsigned o = (jp & 1) ? 0x03020c0cu : 0x0c0c0302u;
      selE[r] = on ? e : 0x0c0c0c0cu; selO[r] = on ? o : 0x0c0c0c0cu;
    }
  }
  f32x4 acc0 = {0.f, 0.f, 0.f, 0.f}, acc1 = {0.f, 0.f, 0.f, 0.f};
#pragma unroll
  for (int i2 = 0; i2 < 8; ++i2) {
    const unsigned s0 = __builtin_amdgcn_update_dpp(pk[i2], pk[i2], SHR4, 0xf, 0xa, false);
    const unsigned s1 = __builtin_amdgcn_update_dpp(pk[i2], pk[i2], SHL4, 0xf, 0x5, false);
    v4u a;
#pragma unroll
    for (int r = 0; r < 4; ++r) a[r] = __builtin_amdgcn_perm(s0, s0, selE[r]);
    acc0 = mfma(a, vf[2 * i2], acc0);
#pragma unroll
    for (int r = 0; r < 4; ++r) a[r] = __builtin_amdgcn_perm(s1, s1, selE[r]);
    acc1 = mfma(a, vf[2 * i2], acc1);
#pragma unroll
    for (int r = 0; r < 4; ++r) a[r] = __builtin_amdgcn_perm(s0, s0, selO[r]);
    acc0 = mfma(a, vf[2 * i2 + 1], acc0);
#pragma unroll
    for (int r = 0; r < 4; ++r) a[r] = __builtin_amdgcn_perm(s1, s1, selO[r]);
    acc1 = mfma(a, vf[2 * i2 + 1], acc1);
  }
  *(f32x4*)(L_o + w * 512 + (g4 >> 1) * 128 + 8 * c16 + 4 * (g4 & 1)) = acc0;
  *(f32x4*)(L_o + w * 512 + (2 + (g4 >> 1)) * 128 + 8 * c16 + 4 * (g4 & 1)) = acc1;
  STAMP(6);
  __syncthreads();
  {
    float o0 = 0.f, o1 = 0.f;
#pragma unroll
    for (int ww = 0; ww < 4; ++ww) { const f32x2 t = *(const f32x2*)(L_o + ww * 512 + w * 128 + 2 * lane); o0 += t.x; o1 += t.y; }
    const v4u g = {g_lo(o0, tlo), thi, g_lo(o1, tlo), thi};
    __builtin_amdgcn_raw_buffer_store_b128(g, rW, WS_O + ((grp * 16 + s) * 4 + w) * 1024 + lane * 16, 0, AUX_SC1);
    if (lane == 0) {
      const f32x4 mm = *(const f32x4*)(L_max + w * 4), ll = *(const f32x4*)(L_sum + w * 4);
      const float M = fmaxf(fmaxf(mm[0], mm[1]), fmaxf(mm[2], mm[3])), Ls = (ll[0] + ll[1]) + (ll[2] + ll[3]);
      const v4u gs = {g_lo(M, tlo) , thi, g_lo(Ls, tlo), thi};
      __builtin_amdgcn_raw_buffer_store_b128(gs, rW, WS_ST + ((grp * 16 + s) * 4 + w) * 16, 0, AUX_SC1);
    }
  }
  SB();
  STAMP(7);
  {
    const int hh = s >> 2, sp = lane >> 2, q4 = lane & 3;
    const int ooff = WS_O + ((grp * 16 + sp) * 4 + hh) * 1024 + (32 * w + 2 * q4) * 8;
    const int soff = WS_ST + ((grp * 16 + sp) * 4 + hh) * 16;
    unsigned wl[16], wh[16];
#pragma unroll
    for (int k = 0; k < 4; ++k)
#pragma unroll
      for (int j = 0; j < 4; ++j) {
        wl[k * 4 + j] = __builtin_amdgcn_perm(wo[2 * k + 1][j], wo[2 * k][j], 0x05040100u);
        wh[k * 4 + j] = __builtin_amdgcn_perm(wo[2 * k + 1][j], wo[2 * k][j], 0x07060302u);
        asm volatile("" : "+v"(wl[k * 4 + j]), "+v"(wh[k * 4 + j]));
      }
    SB();
    v4u od[4], sd;
    for (unsigned spins = 0;; ++spins) {
#pragma unroll
      for (int k = 0; k < 4; ++k) od[k] = __builtin_amdgcn_raw_buffer_load_b128(rW, ooff, k * 64, AUX_SC1);
      sd = __builtin_amdgcn_raw_buffer_load_b128(rW, soff, 0, AUX_SC1);
      unsigned bad = (sd.y ^ thi) | (sd.w ^ thi), badl = (sd.x ^ tlo) | (sd.z ^ tlo);
#pragma unroll
      for (int k = 0; k < 4; ++k) { bad |= (od[k].y ^ thi) | (od[k].w ^ thi); badl |= (od[k].x ^ tlo) | (od[k].z ^ tlo); }
      bad |= badl & 0xffu;
      if (__all(bad == 0u) || spins >= SPIN_CAP) break;
      __builtin_amdgcn_s_sleep(1);
    }
    STAMP(8);
    const float ms = g_val(sd.x), ls = g_val(sd.z);
    float M = fmaxf(ms, xor4(ms)); M = fmaxf(M, dpp<ROR8>(M)); M = max16(M); M = max32(M);
    const float wgt = __builtin_amdgcn_exp2f((ms - M) * LOG2E);
    float L = wgt * ls; L += xor4(L); L += dpp<ROR8>(L); L = sum16(L); L = sum32(L);
    float v8[8];
#pragma unroll
    for (int k = 0; k < 4; ++k) { v8[2 * k] = g_val(od[k].x) * wgt; v8[2 * k + 1] = g_val(od[k].z) * wgt; }
    float a4[4], a2[2];
#pragma unroll
    for (int j = 0; j < 4; ++j) a4[j] = tr32(v8[j], v8[j + 4]);
#pragma unroll
    for (int j = 0; j < 2; ++j) a2[j] = tr16(a4[j], a4[j + 2]);
    const float t0 = a2[0] + dpp<ROR8>(a2[0]), t1 = a2[1] + dpp<ROR8>(a2[1]);
    float a1 = (lane & 8) ? t1 : t0;
    a1 += xor4(a1);
    const int idx = ((lane >> 3) & 1) + 2 * ((lane >> 4) & 1) + 4 * (lane >> 5);
    const int dim = 8 * (idx >> 1) + 2 * q4 + (idx & 1);
    const float ov = a1 / L;
    if (!(lane & 4)) L_ob[w * 32 + dim] = (unsigned short)(cvtpk(ov, ov) & 0xffffu);
    __builtin_amdgcn_fence(__ATOMIC_RELEASE, "wavefront");
    __builtin_amdgcn_wave_barrier();
    const v4u of = *(const v4u*)(L_ob + w * 32 + 8 * (lane >> 4));
    float acc[8];
#pragma unroll
    for (int j = 0; j < 8; ++j) acc[j] = 0.f;
#pragma unroll
    for (int k = 0; k < 4; ++k)
#pragma unroll
      for (int j = 0; j < 4; ++j) {
        acc[2 * j] = dot2(wl[k * 4 + j], of[k], acc[2 * j]);
        acc[2 * j + 1] = dot2(wh[k * 4 + j], of[k], acc[2 * j + 1]);
      }
#pragma unroll
    for (int j = 0; j < 4; ++j) a4[j] = tr32(acc[j], acc[j + 4]);
#pragma unroll
    for (int j = 0; j < 2; ++j) a2[j] = tr16(a4[j], a4[j + 2]);
    const int colb = 8 * (lane & 15) + 2 * ((lane >> 4) & 1) + 4 * (lane >> 5);
    *(f32x2*)(L_f + w * 128 + colb) = f32x2{a2[0], a2[1]};
  }
  __syncthreads();
  if (w == 0) {
    float f0 = 0.f, f1 = 0.f;
#pragma unroll
    for (int ww = 0; ww < 4; ++ww) { const f32x2 t = *(const f32x2*)(L_f + ww * 128 + 2 * lane); f0 += t.x; f1 += t.y; }
    const v4u g = {g_lo(f0, tlo), thi, g_lo(f1, tlo), thi};
    __builtin_amdgcn_raw_buffer_store_b128(g, rW, WS_F + (br * 32 + kvh * 4 + (s >> 2)) * 4096 + ((s & 3) * 128 + 2 * lane) * 8, 0, AUX_SC1);
    SB();
    STAMP(9);
    v4u fd;
    for (unsigned spins = 0;; ++spins) {
      fd = __builtin_amdgcn_raw_buffer_load_b128(rW, WS_F + lane * 4096 + b * 16, 0, AUX_SC1);
      const unsigned bad = (fd.y ^ thi) | (fd.w ^ thi) | (((fd.x ^ tlo) | (fd.z ^ tlo)) & 0xffu);
      if (__all(bad == 0u) || spins >= SPIN_CAP) break;
      __builtin_amdgcn_s_sleep(1);
    }
    STAMP(10);
    float s0 = g_val(fd.x), s1 = g_val(fd.z);
    s0 += dpp<XOR1>(s0); s1 += dpp<XOR1>(s1);
    s0 += dpp<XOR2>(s0); s1 += dpp<XOR2>(s1);
    s0 += dpp<HMIR>(s0); s1 += dpp<HMIR>(s1);
    s0 += dpp<MIR>(s0); s1 += dpp<MIR>(s1);
    s0 = sum16(s0); s1 = sum16(s1);
    const unsigned ab = cvtpk(s0, s1);
    const unsigned aA = __builtin_amdgcn_readlane(ab, 0), aB = __builtin_amdgcn_readlane(ab, 32);
    const float omg = rbf(1.f - gate);
    const unsigned ga2 = cvtpk(gate * bflo(aA), gate * bfhi(aA)), gb2 = cvtpk(omg * bflo(aB), omg * bfhi(aB));
    const unsigned res = cvtpk(bflo(ga2) + bflo(gb2), bfhi(ga2) + bfhi(gb2));
    if (lane == 0) {
      ((unsigned*)p.out)[b] = res;
      if (b == 0) *(u64*)(p.ws + WS_EPOCH) = e64;
    }
  }
  STAMP(11);
}

extern "C" void kernel_launch(void* const* d_in, const int* in_sizes, int n_in, void* d_out, int out_size, void* d_ws, size_t ws_size, hipStream_t stream) {
  Params p;
  p.x = (const unsigned short*)d_in[0]; p.kA = (const unsigned short*)d_in[1]; p.vA = (const unsigned short*)d_in[2];
  p.wqA = (const unsigned short*)d_in[3]; p.woA = (const unsigned short*)d_in[4];
  p.kB = (const unsigned short*)d_in[6]; p.vB = (const unsigned short*)d_in[7];
  p.wqB = (const unsigned short*)d_in[8]; p.woB = (const unsigned short*)d_in[9]; p.gw = (const unsigned short*)d_in[10];
  p.out = (unsigned short*)d_out; p.ws = (unsigned char*)d_ws;
  hipFuncSetAttribute((const void*)fused_decode, hipFuncAttributeMaxDynamicSharedMemorySize, LDS_BYTES);
  fused_decode<<<256, 256, LDS_BYTES, stream>>>(p);
}
```

```cpp
#include <hip/hip_runtime.h>
#include <stdint.h>

typedef unsigned v4u __attribute__((ext_vector_type(4)));
typedef unsigned v2u __attribute__((ext_vector_type(2)));
typedef float f32x4 __attribute__((ext_vector_type(4)));
typedef float f32x2 __attribute__((ext_vector_type(2)));
typedef short bf16x8 __attribute__((ext_vector_type(8)));
typedef __bf16 bf16x2 __attribute__((ext_vector_type(2)));
typedef unsigned long long u64;
#define GAS __attribute__((address_space(1)))
#define RLX_AGENT __ATOMIC_RELAXED, __HIP_MEMORY_SCOPE_AGENT
#define SB() __builtin_amdgcn_sched_barrier(0)

constexpr int SK = 32768, WIN = 4096, DH = 128;
constexpr int AUX_NT = 2, AUX_SC1 = 16, RS_FLAGS = 0x00020000;
constexpr int WS_EPOCH = 0;
constexpr int WS_Q = 4096;
constexpr int WS_ST = WS_Q + 16 * 16 * 512;
constexpr int WS_O = WS_ST + 16 * 16 * 4 * 16;
constexpr int WS_F = WS_O + 16 * 16 * 4 * 128 * 8;
constexpr int WS_END = WS_F + 64 * 512 * 8;
constexpr int WS_STAMP = 4 << 20;
constexpr unsigned SPIN_CAP = 1u << 14;
constexpr int LDS_BYTES = 16 * 1024;
#define K1 4
#define V1 4

struct Params {
  const unsigned short *x, *kA, *vA, *wqA, *woA, *kB, *vB, *wqB, *woB, *gw;
  unsigned short* out;
  unsigned char* ws;
};

__device__ __forceinline__ float dot2(unsigned a, unsigned b, float c) {
  return __builtin_amdgcn_fdot2_f32_bf16(__builtin_bit_cast(bf16x2, a), __builtin_bit_cast(bf16x2, b), c, false);
}
__device__ __forceinline__ unsigned cvtpk(float lo, float hi) {
  bf16x2 r = {(__bf16)lo, (__bf16)hi};
  return __builtin_bit_cast(unsigned, r);
}
__device__ __forceinline__ float bflo(unsigned u) { return __uint_as_float(u << 16); }
__device__ __forceinline__ float bfhi(unsigned u) { return __uint_as_float(u & 0xffff0000u); }
__device__ __forceinline__ float rbf(float x) { return bflo(cvtpk(x, x) & 0xffffu); }
template <int CTRL> __device__ __forceinline__ float dpp(float x) {
  return __int_as_float(__builtin_amdgcn_mov_dpp(__float_as_int(x), CTRL, 0xf, 0xf, true));
}
constexpr int XOR1 = 0xB1, XOR2 = 0x4E, HMIR = 0x141, MIR = 0x140, ROR8 = 0x128, SHL4 = 0x104, SHR4 = 0x114;
__device__ __forceinline__ float xor4(float x) {
  int a = __builtin_amdgcn_update_dpp(0, __float_as_int(x), SHL4, 0xf, 0x5, false);
  a = __builtin_amdgcn_update_dpp(a, __float_as_int(x), SHR4, 0xf, 0xa, false);
  return __int_as_float(a);
}
__device__ __forceinline__ float tr32(float a, float b) {
  auto r = __builtin_amdgcn_permlane32_swap(__float_as_uint(a), __float_as_uint(b), false, false);
  return __uint_as_float(r[0]) + __uint_as_float(r[1]);
}
__device__ __forceinline__ float tr16(float a, float b) {
  auto r = __builtin_amdgcn_permlane16_swap(__float_as_uint(a), __float_as_uint(b), false, false);
  return __uint_as_float(r[0]) + __uint_as_float(r[1]);
}
__device__ __forceinline__ float max32(float a) {
  auto r = __builtin_amdgcn_permlane32_swap(__float_as_uint(a), __float_as_uint(a), false, false);
  return fmaxf(__uint_as_float(r[0]), __uint_as_float(r[1]));
}
__device__ __forceinline__ float max16(float a) {
  auto r = __builtin_amdgcn_permlane16_swap(__float_as_uint(a), __float_as_uint(a), false, false);
  return fmaxf(__uint_as_float(r[0]), __uint_as_float(r[1]));
}
__device__ __forceinline__ float sum32(float a) { return tr32(a, a); }
__device__ __forceinline__ float sum16(float a) { return tr16(a, a); }
__device__ __forceinline__ f32x4 mfma(v4u a, v4u b, f32x4 c) {
  return __builtin_amdgcn_mfma_f32_16x16x32_bf16(__builtin_bit_cast(bf16x8, a), __builtin_bit_cast(bf16x8, b), c, 0, 0, 0);
}
#define STAMP(i)

__device__ __forceinline__ unsigned g_lo(float v, unsigned tl) { return ((__float_as_uint(v) + 0x80u) & 0xffffff00u) | tl; }
__device__ __forceinline__ float g_val(unsigned lo) { return __uint_as_float(lo & 0xffffff00u); }

__global__ void __launch_bounds__(256, 1) fused_decode(Params p) {
  extern __shared__ __attribute__((aligned(16))) unsigned char smem[];
  float* lds = (float*)smem;
  float* L_qred = lds;
  float* L_max = lds + 256;
  float* L_sum = lds + 272;
  unsigned* L_q = (unsigned*)(lds + 288);
  float* L_o = lds + 544;
  unsigned short* L_ob = (unsigned short*)(lds + 544 + 2048);
  float* L_f = lds + 544 + 2048 + 64;
  unsigned long long tstart; asm volatile("s_memrealtime %0\n s_waitcnt lgkmcnt(0)" : "=s"(tstart)::"memory");
  __builtin_amdgcn_sched_barrier(0);
  const int b = blockIdx.x, tid = threadIdx.x, lane = tid & 63;
  const int w = __builtin_amdgcn_readfirstlane(tid >> 6);
  const int kvh = b & 7, br = b >> 7, s = (b >> 3) & 15, grp = br * 8 + kvh;
  const unsigned short* kp = (br ? p.kB : p.kA) + ((size_t)kvh * SK + (SK - WIN) + s * 256) * DH;
  const unsigned short* vp = (br ? p.vB : p.vA) + ((size_t)kvh * SK + (SK - WIN) + s * 256) * DH;
  const unsigned short* wqp = (br ? p.wqB : p.wqA) + (size_t)(s & 1) * 256 * 4096 + kvh * 512 + (s >> 1) * 64;
  const unsigned short* wop = (br ? p.woB : p.woA) + (size_t)(kvh * 512 + (s >> 2) * 128) * 512 + (s & 3) * 128;
  const __amdgpu_buffer_rsrc_t rK = __builtin_amdgcn_make_buffer_rsrc((void*)kp, 0, 65536, RS_FLAGS);
  const __amdgpu_buffer_rsrc_t rV = __builtin_amdgcn_make_buffer_rsrc((void*)vp, 0, 65536, RS_FLAGS);
  const __amdgpu_buffer_rsrc_t rQ = __builtin_amdgcn_make_buffer_rsrc((void*)wqp, 0, 255 * 8192 + 128, RS_FLAGS);
  const __amdgpu_buffer_rsrc_t rO = __builtin_amdgcn_make_buffer_rsrc((void*)wop, 0, 127 * 1024 + 256, RS_FLAGS);
  const __amdgpu_buffer_rsrc_t rW = __builtin_amdgcn_make_buffer_rsrc((void*)p.ws, 0, WS_END, RS_FLAGS);
  const __amdgpu_buffer_rsrc_t rX = __builtin_amdgcn_make_buffer_rsrc((void*)p.x, 0, 1024, RS_FLAGS);
  const __amdgpu_buffer_rsrc_t rG = __builtin_amdgcn_make_buffer_rsrc((void*)p.gw, 0, 1024, RS_FLAGS);

  const v2u ep = __builtin_amdgcn_raw_buffer_load_b64(rW, WS_EPOCH, 0, AUX_SC1);
  const v4u xf = __builtin_amdgcn_raw_buffer_load_b128(rX, ((s & 1) * 256 + 64 * w + 8 * (lane >> 3)) * 2, 0, 0);
  const v4u xa = __builtin_amdgcn_raw_buffer_load_b128(rX, lane * 16, 0, 0);
  const v4u ga = __builtin_amdgcn_raw_buffer_load_b128(rG, lane * 16, 0, 0);
  SB();
  v4u wq[8];
  {
    const int vo = (64 * w + 8 * (lane >> 3)) * 8192 + (lane & 7) * 16;
#pragma unroll
    for (int i = 0; i < 8; ++i) wq[i] = __builtin_amdgcn_raw_buffer_load_b128(rQ, vo, i * 8192, AUX_NT);
  }
  SB();
  v4u kf[16], vf[16], wo[8];
  const int kvo = w * 16384 + ((lane & 15) >> 2) * 256 + (lane >> 4) * 64 + (lane & 3) * 16;
  const int vvo = w * 16384 + lane * 16;
  const unsigned rot = (((unsigned)(b * 4 + w) * 2654435761u) >> 16);
#define KVOFF(i) ((int)(((i) + rot) & 15u) * 1024)
#pragma unroll
  for (int i = 0; i < K1; ++i) kf[i] = __builtin_amdgcn_raw_buffer_load_b128(rK, kvo, KVOFF(i), AUX_NT);
  SB();
  const u64 e64 = (((u64)ep.y << 32) | ep.x) + 1ull;
  const unsigned tlo = (unsigned)e64 & 0xffu, thi = (unsigned)(e64 >> 8);
  float gate;
  {
    float gs = 0.f;
#pragma unroll
    for (int j = 0; j < 4; ++j) {
      const unsigned pr = cvtpk(bflo(xa[j]) * bflo(ga[j]), bfhi(xa[j]) * bfhi(ga[j]));
      gs += bflo(pr) + bfhi(pr);
    }
    gs += dpp<XOR1>(gs); gs += dpp<XOR2>(gs); gs += dpp<HMIR>(gs); gs += dpp<MIR>(gs);
    gs = sum16(gs); gs = sum32(gs);
    gs = rbf(gs);
    gate = rbf(1.f / (1.f + __expf(-gs)));
    asm volatile("" : "+v"(gate));
  }
  SB();
  {
    float acc[8];
#pragma unroll
    for (int j = 0; j < 8; ++j) acc[j] = 0.f;
#pragma unroll
    for (int k = 0; k < 4; ++k) {
#pragma unroll
      for (int j = 0; j < 4; ++j) {
        const unsigned lo = __builtin_amdgcn_perm(wq[2 * k + 1][j], wq[2 * k][j], 0x05040100u);
        const unsigned hi = __builtin_amdgcn_perm(wq[2 * k + 1][j], wq[2 * k][j], 0x07060302u);
        acc[2 * j] = dot2(lo, xf[k], acc[2 * j]);
        acc[2 * j + 1] = dot2(hi, xf[k], acc[2 * j + 1]);
      }
      SB();
      kf[K1 + 2 * k] = __builtin_amdgcn_raw_buffer_load_b128(rK, kvo, KVOFF(K1 + 2 * k), AUX_NT);
      kf[K1 + 2 * k + 1] = __builtin_amdgcn_raw_buffer_load_b128(rK, kvo, KVOFF(K1 + 2 * k + 1), AUX_NT);
      SB();
    }
    float a4[4], a2[2];
#pragma unroll
    for (int j = 0; j < 4; ++j) a4[j] = tr32(acc[j], acc[j + 4]);
#pragma unroll
    for (int j = 0; j < 2; ++j) a2[j] = tr16(a4[j], a4[j + 2]);
    const float t0 = a2[0] + dpp<ROR8>(a2[0]), t1 = a2[1] + dpp<ROR8>(a2[1]);
    const float a1 = (lane & 8) ? t1 : t0;
    const int col = 8 * (lane & 7) + ((lane >> 3) & 1) + 2 * ((lane >> 4) & 1) + 4 * (lane >> 5);
    L_qred[w * 64 + col] = a1;
  }
  STAMP(1);
  __syncthreads();
  if (w == 0 && lane < 32) {
    float q0 = 0.f, q1 = 0.f;
#pragma unroll
    for (int ww = 0; ww < 4; ++ww) { const f32x2 t = *(const f32x2*)(L_qred + ww * 64 + 2 * lane); q0 += t.x; q1 += t.y; }
    const v4u g = {g_lo(q0, tlo), thi, g_lo(q1, tlo), thi};
    __builtin_amdgcn_raw_buffer_store_b128(g, rW, WS_Q + (grp * 16 + s) * 512 + lane * 16, 0, AUX_SC1);
  }
  SB();
  STAMP(2);
#pragma unroll
  for (int i = K1 + 8; i < 16; ++i) kf[i] = __builtin_amdgcn_raw_buffer_load_b128(rK, kvo, KVOFF(i), AUX_NT);
  SB();
#pragma unroll
  for (int i = 0; i < V1; ++i) vf[i] = __builtin_amdgcn_raw_buffer_load_b128(rV, vvo, KVOFF(i), AUX_NT);
  SB();
  const int c16 = lane & 15, g4 = lane >> 4;
  const int hc = (c16 >> 3) + 2 * ((c16 >> 2) & 1);
  const int qoff = WS_Q + (grp * 16 + 4 * w + 2 * (lane >> 5)) * 512 + (lane & 31) * 16;
  v4u qa = __builtin_amdgcn_raw_buffer_load_b128(rW, qoff, 0, AUX_SC1);
  v4u qb = __builtin_amdgcn_raw_buffer_load_b128(rW, qoff, 512, AUX_SC1);
  SB();
#pragma unroll
  for (int i = V1; i < 16; ++i) vf[i] = __builtin_amdgcn_raw_buffer_load_b128(rV, vvo, KVOFF(i), AUX_NT);
  SB();
  STAMP(3);
#define QBAD() ((qa.y ^ thi) | (qa.w ^ thi) | (qb.y ^ thi) | (qb.w ^ thi) | (((qa.x ^ tlo) | (qa.z ^ tlo) | (qb.x ^ tlo) | (qb.z ^ tlo)) & 0xffu))
  if (!__all(QBAD() == 0u)) {
    for (unsigned spins = 0; spins < SPIN_CAP; ++spins) {
      qa = __builtin_amdgcn_raw_buffer_load_b128(rW, qoff, 0, AUX_SC1);
      qb = __builtin_amdgcn_raw_buffer_load_b128(rW, qoff, 512, AUX_SC1);
      if (__all(QBAD() == 0u)) break;
      __builtin_amdgcn_s_sleep(1);
    }
  }
  L_q[w * 64 + lane] = cvtpk(g_val(qa.x) + g_val(qb.x), g_val(qa.z) + g_val(qb.z));
  STAMP(4);
  __syncthreads();
  const v4u qf = *(const v4u*)(L_q + hc * 64 + 4 * (4 * g4 + (c16 & 3)));
  float sc[16];
  const bool b0 = lane & 1, b1 = lane & 2;
#pragma unroll
  for (int i = 0; i < 16; ++i) {
    const f32x4 d = mfma(kf[i], qf, f32x4{0.f, 0.f, 0.f, 0.f});
    const float t0 = b0 ? d[1] : d[0], t1 = b0 ? d[3] : d[2];
    float v = b1 ? t1 : t0;
    v += dpp<XOR1>(v); v += dpp<XOR2>(v);
    sc[i] = v;
  }
#pragma unroll
  for (int i = 0; i < 16; i += 2) {
    const unsigned r1 = cvtpk(sc[i], sc[i + 1]);
    const unsigned r2 = cvtpk(bflo(r1) * 0.08838834764831845f, bfhi(r1) * 0.08838834764831845f);
    sc[i] = bflo(r2); sc[i + 1] = bfhi(r2);
  }
  float m = sc[0];
#pragma unroll
  for (int i = 1; i < 16; ++i) m = fmaxf(m, sc[i]);
  m = max16(m); m = max32(m);
  if (lane < 16 && !(lane & 3)) L_max[hc * 4 + w] = m;
  __syncthreads();
  {
    const f32x4 mm = *(const f32x4*)(L_max + hc * 4);
    m = fmaxf(fmaxf(mm[0], mm[1]), fmaxf(mm[2], mm[3]));
  }
  float l = 0.f;
  unsigned pk[8];
  constexpr float LOG2E = 1.4426950408889634f;
  const float mb = m * LOG2E;
#pragma unroll
  for (int i = 0; i < 16; i += 2) {
    const float p0 = __builtin_amdgcn_exp2f(sc[i] * LOG2E - mb), p1 = __builtin_amdgcn_exp2f(sc[i + 1] * LOG2E - mb);
    l += p0 + p1;
    pk[i >> 1] = cvtpk(p0, p1);
  }
  l = sum16(l); l = sum32(l);
  if (lane < 16 && !(lane & 3)) L_sum[hc * 4 + w] = l;
  SB();
  {
    const int vo = (32 * w + 8 * (lane >> 4)) * 1024 + (lane & 15) * 16;
#pragma unroll
    for (int i = 0; i < 8; ++i) wo[i] = __builtin_amdgcn_raw_buffer_load_b128(rO, vo, i * 1024, AUX_NT);
  }
  SB();
  STAMP(5);
  unsigned selE[4], selO[4];
  {
    const int jp = lane & 7;
#pragma unroll
    for (int r = 0; r < 4; ++r) {
      const bool on = (jp >> 1) == r;
      const unsigned e = (jp & 1) ? 0x01000c0cu : 0x0c0c0100u;
      const unsigned o = (jp & 1) ? 0x03020c0cu : 0x0c0c0302u;
      selE[r] = on ? e : 0x0c0c0c0cu; selO[r] = on ? o : 0x0c0c0c0cu;
    }
  }
  f32x4 acc0 = {0.f, 0.f, 0.f, 0.f}, acc1 = {0.f, 0.f, 0.f, 0.f};
#pragma unroll
  for (int i2 = 0; i2 < 8; ++i2) {
    const unsigned s0 = __builtin_amdgcn_update_dpp(pk[i2], pk[i2], SHR4, 0xf, 0xa, false);
    const unsigned s1 = __builtin_amdgcn_update_dpp(pk[i2], pk[i2], SHL4, 0xf, 0x5, false);
    v4u a;
#pragma unroll
    for (int r = 0; r < 4; ++r) a[r] = __builtin_amdgcn_perm(s0, s0, selE[r]);
    acc0 = mfma(a, vf[2 * i2], acc0);
#pragma unroll
    for (int r = 0; r < 4; ++r) a[r] = __builtin_amdgcn_perm(s1, s1, selE[r]);
    acc1 = mfma(a, vf[2 * i2], acc1);
#pragma unroll
    for (int r = 0; r < 4; ++r) a[r] = __builtin_amdgcn_perm(s0, s0, selO[r]);
    acc0 = mfma(a, vf[2 * i2 + 1], acc0);
#pragma unroll
    for (int r = 0; r < 4; ++r) a[r] = __builtin_amdgcn_perm(s1, s1, selO[r]);
    acc1 = mfma(a, vf[2 * i2 + 1], acc1);
  }
  *(f32x4*)(L_o + w * 512 + (g4 >> 1) * 128 + 8 * c16 + 4 * (g4 & 1)) = acc0;
  *(f32x4*)(L_o + w * 512 + (2 + (g4 >> 1)) * 128 + 8 * c16 + 4 * (g4 & 1)) = acc1;
  STAMP(6);
  __syncthreads();
  {
    float o0 = 0.f, o1 = 0.f;
#pragma unroll
    for (int ww = 0; ww < 4; ++ww) { const f32x2 t = *(const f32x2*)(L_o + ww * 512 + w * 128 + 2 * lane); o0 += t.x; o1 += t.y; }
    const v4u g = {g_lo(o0, tlo), thi, g_lo(o1, tlo), thi};
    __builtin_amdgcn_raw_buffer_store_b128(g, rW, WS_O + ((grp * 16 + s) * 4 + w) * 1024 + lane * 16, 0, AUX_SC1);
    if (lane == 0) {
      const f32x4 mm = *(const f32x4*)(L_max + w * 4), ll = *(const f32x4*)(L_sum + w * 4);
      const float M = fmaxf(fmaxf(mm[0], mm[1]), fmaxf(mm[2], mm[3])), Ls = (ll[0] + ll[1]) + (ll[2] + ll[3]);
      const v4u gs = {g_lo(M, tlo) , thi, g_lo(Ls, tlo), thi};
      __builtin_amdgcn_raw_buffer_store_b128(gs, rW, WS_ST + ((grp * 16 + s) * 4 + w) * 16, 0, AUX_SC1);
    }
  }
  SB();
  STAMP(7);
  {
    const int hh = s >> 2, sp = lane >> 2, q4 = lane & 3;
    const int ooff = WS_O + ((grp * 16 + sp) * 4 + hh) * 1024 + (32 * w + 2 * q4) * 8;
    const int soff = WS_ST + ((grp * 16 + sp) * 4 + hh) * 16;
    unsigned wl[16], wh[16];
#pragma unroll
    for (int k = 0; k < 4; ++k)
#pragma unroll
      for (int j = 0; j < 4; ++j) {
        wl[k * 4 + j] = __builtin_amdgcn_perm(wo[2 * k + 1][j], wo[2 * k][j], 0x05040100u);
        wh[k * 4 + j] = __builtin_amdgcn_perm(wo[2 * k + 1][j], wo[2 * k][j], 0x07060302u);
        asm volatile("" : "+v"(wl[k * 4 + j]), "+v"(wh[k * 4 + j]));
      }
    SB();
    v4u od[4], sd;
    for (unsigned spins = 0;; ++spins) {
#pragma unroll
      for (int k = 0; k < 4; ++k) od[k] = __builtin_amdgcn_raw_buffer_load_b128(rW, ooff, k * 64, AUX_SC1);
      sd = __builtin_amdgcn_raw_buffer_load_b128(rW, soff, 0, AUX_SC1);
      unsigned bad = (sd.y ^ thi) | (sd.w ^ thi), badl = (sd.x ^ tlo) | (sd.z ^ tlo);
#pragma unroll
      for (int k = 0; k < 4; ++k) { bad |= (od[k].y ^ thi) | (od[k].w ^ thi); badl |= (od[k].x ^ tlo) | (od[k].z ^ tlo); }
      bad |= badl & 0xffu;
      if (__all(bad == 0u) || spins >= SPIN_CAP) break;
      __builtin_amdgcn_s_sleep(1);
    }
    STAMP(8);
    const float ms = g_val(sd.x), ls = g_val(sd.z);
    float M = fmaxf(ms, xor4(ms)); M = fmaxf(M, dpp<ROR8>(M)); M = max16(M); M = max32(M);
    const float wgt = __builtin_amdgcn_exp2f((ms - M) * LOG2E);
    float L = wgt * ls; L += xor4(L); L += dpp<ROR8>(L); L = sum16(L); L = sum32(L);
    float v8[8];
#pragma unroll
    for (int k = 0; k < 4; ++k) { v8[2 * k] = g_val(od[k].x) * wgt; v8[2 * k + 1] = g_val(od[k].z) * wgt; }
    float a4[4], a2[2];
#pragma unroll
    for (int j = 0; j < 4; ++j) a4[j] = tr32(v8[j], v8[j + 4]);
#pragma unroll
    for (int j = 0; j < 2; ++j) a2[j] = tr16(a4[j], a4[j + 2]);
    const float t0 = a2[0] + dpp<ROR8>(a2[0]), t1 = a2[1] + dpp<ROR8>(a2[1]);
    float a1 = (lane & 8) ? t1 : t0;
    a1 += xor4(a1);
    const int idx = ((lane >> 3) & 1) + 2 * ((lane >> 4) & 1) + 4 * (lane >> 5);
    const int dim = 8 * (idx >> 1) + 2 * q4 + (idx & 1);
    const float ov = a1 / L;
    if (!(lane & 4)) L_ob[w * 32 + dim] = (unsigned short)(cvtpk(ov, ov) & 0xffffu);
    __builtin_amdgcn_fence(__ATOMIC_RELEASE, "wavefront");
    __builtin_amdgcn_wave_barrier();
    const v4u of = *(const v4u*)(L_ob + w * 32 + 8 * (lane >> 4));
    float acc[8];
#pragma unroll
    for (int j = 0; j < 8; ++j) acc[j] = 0.f;
#pragma unroll
    for (int k = 0; k < 4; ++k)
#pragma unroll
      for (int j = 0; j < 4; ++j) {
        acc[2 * j] = dot2(wl[k * 4 + j], of[k], acc[2 * j]);
        acc[2 * j + 1] = dot2(wh[k * 4 + j], of[k], acc[2 * j + 1]);
      }
#pragma unroll
    for (int j = 0; j < 4; ++j) a4[j] = tr32(acc[j], acc[j + 4]);
#pragma unroll
    for (int j = 0; j < 2; ++j) a2[j] = tr16(a4[j], a4[j + 2]);
    const int colb = 8 * (lane & 15) + 2 * ((lane >> 4) & 1) + 4 * (lane >> 5);
    *(f32x2*)(L_f + w * 128 + colb) = f32x2{a2[0], a2[1]};
  }
  __syncthreads();
  if (w == 0) {
    float f0 = 0.f, f1 = 0.f;
#pragma unroll
    for (int ww = 0; ww < 4; ++ww) { const f32x2 t = *(const f32x2*)(L_f + ww * 128 + 2 * lane); f0 += t.x; f1 += t.y; }
    const v4u g = {g_lo(f0, tlo), thi, g_lo(f1, tlo), thi};
    __builtin_amdgcn_raw_buffer_store_b128(g, rW, WS_F + (br * 32 + kvh * 4 + (s >> 2)) * 4096 + ((s & 3) * 128 + 2 * lane) * 8, 0, AUX_SC1);
    SB();
    STAMP(9);
    v4u fd;
    for (unsigned spins = 0;; ++spins) {
      fd = __builtin_amdgcn_raw_buffer_load_b128(rW, WS_F + lane * 4096 + b * 16, 0, AUX_SC1);
      const unsigned bad = (fd.y ^ thi) | (fd.w ^ thi) | (((fd.x ^ tlo) | (fd.z ^ tlo)) & 0xffu);
      if (__all(bad == 0u) || spins >= SPIN_CAP) break;
      __builtin_amdgcn_s_sleep(1);
    }
    STAMP(10);
    float s0 = g_val(fd.x), s1 = g_val(fd.z);
    s0 += dpp<XOR1>(s0); s1 += dpp<XOR1>(s1);
    s0 += dpp<XOR2>(s0); s1 += dpp<XOR2>(s1);
    s0 += dpp<HMIR>(s0); s1 += dpp<HMIR>(s1);
    s0 += dpp<MIR>(s0); s1 += dpp<MIR>(s1);
    s0 = sum16(s0); s1 = sum16(s1);
    const unsigned ab = cvtpk(s0, s1);
    const unsigned aA = __builtin_amdgcn_readlane(ab, 0), aB = __builtin_amdgcn_readlane(ab, 32);
    const float omg = rbf(1.f - gate);
    const unsigned ga2 = cvtpk(gate * bflo(aA), gate * bfhi(aA)), gb2 = cvtpk(omg * bflo(aB), omg * bfhi(aB));
    const unsigned res = cvtpk(bflo(ga2) + bflo(gb2), bfhi(ga2) + bfhi(gb2));
    if (lane == 0) {
      ((unsigned*)p.out)[b] = res;
      if (b == 0) *(u64*)(p.ws + WS_EPOCH) = e64;
    }
  }
  while (__builtin_amdgcn_s_memrealtime() - tstart < 4000ull) __builtin_amdgcn_s_sleep(2);
}

extern "C" void kernel_launch(void* const* d_in, const int* in_sizes, int n_in, void* d_out, int out_size, void* d_ws, size_t ws_size, hipStream_t stream) {
  Params p;
  p.x = (const unsigned short*)d_in[0]; p.kA = (const unsigned short*)d_in[1]; p.vA = (const unsigned short*)d_in[2];
  p.wqA = (const unsigned short*)d_in[3]; p.woA = (const unsigned short*)d_in[4];
  p.kB = (const unsigned short*)d_in[6]; p.vB = (const unsigned short*)d_in[7];
  p.wqB = (const unsigned short*)d_in[8]; p.woB = (const unsigned short*)d_in[9]; p.gw = (const unsigned short*)d_in[10];
  p.out = (unsigned short*)d_out; p.ws = (unsigned char*)d_ws;
  hipFuncSetAttribute((const void*)fused_decode, hipFuncAttributeMaxDynamicSharedMemorySize, LDS_BYTES);
  fused_decode<<<256, 256, LDS_BYTES, stream>>>(p);
}
```

```cpp
#include <hip/hip_runtime.h>
#include <stdint.h>

typedef unsigned v4u __attribute__((ext_vector_type(4)));
typedef unsigned v2u __attribute__((ext_vector_type(2)));
typedef float f32x4 __attribute__((ext_vector_type(4)));
typedef float f32x2 __attribute__((ext_vector_type(2)));
typedef short bf16x8 __attribute__((ext_vector_type(8)));
typedef short bf16x4 __attribute__((ext_vector_type(4)));
typedef __bf16 bf16x2 __attribute__((ext_vector_type(2)));
typedef unsigned long long u64;
#define SB() __builtin_amdgcn_sched_barrier(0)

constexpr int SK = 32768, WIN = 4096, DH = 128;
constexpr int AUX_NT = 2, RS_FLAGS = 0x00020000;
#define HLD 16
#define HST 16
constexpr int AUX_SC1 = HLD, AUX_ST = HST;
constexpr int WS_EPOCH = 0;
constexpr int WS_Q = 4096;
constexpr int MSG = 384;
constexpr int WS_O = WS_Q + 16 * 16 * 512;
constexpr int WS_F = WS_O + 16 * 16 * 16 * MSG;
constexpr int WS_END = WS_F + 256 * 512 * 8;
constexpr int WS_STAMP = 4 << 20;
constexpr unsigned SPIN_CAP = 1u << 14;
constexpr int LDS_BYTES = 96 * 1024;
#define WOB 4
#define ODELAY 4
#define FDELAY 4
#define OACK 0
#define OSLEEP 2
#define FACK 0
#define PRETOUCH 0
#define WND 3
#define KDELAY 0
#define QD1 8
#define QD2 20
#define PRIO 1
#define OWAIT 2

#define WSEND 0
static inline size_t ws_off(size_t ws_size) { return WSEND ? ((ws_size - (size_t)(WSEND) * (8u << 20)) & ~(size_t)0xfffff) : 0; }
struct Params {
  const unsigned short *x, *kA, *vA, *wqA, *woA, *kB, *vB, *wqB, *woB, *gw;
  unsigned short* out;
  unsigned char* ws;
};

__device__ __forceinline__ float dot2(unsigned a, unsigned b, float c) {
  return __builtin_amdgcn_fdot2_f32_bf16(__builtin_bit_cast(bf16x2, a), __builtin_bit_cast(bf16x2, b), c, false);
}
__device__ __forceinline__ unsigned cvtpk(float lo, float hi) {
  bf16x2 r = {(__bf16)lo, (__bf16)hi};
  return __builtin_bit_cast(unsigned, r);
}
__device__ __forceinline__ float bflo(unsigned u) { return __uint_as_float(u << 16); }
__device__ __forceinline__ float bfhi(unsigned u) { return __uint_as_float(u & 0xffff0000u); }
__device__ __forceinline__ float rbf(float x) { return bflo(cvtpk(x, x)); }
template <int CTRL> __device__ __forceinline__ float dpp(float x) {
  return __int_as_float(__builtin_amdgcn_mov_dpp(__float_as_int(x), CTRL, 0xf, 0xf, true));
}
template <int CTRL> __device__ __forceinline__ unsigned dppu(unsigned x) {
  return (unsigned)__builtin_amdgcn_mov_dpp((int)x, CTRL, 0xf, 0xf, true);
}
constexpr int XOR1 = 0xB1, XOR2 = 0x4E, HMIR = 0x141, MIR = 0x140, ROR8 = 0x128, SHL4 = 0x104, SHR4 = 0x114;
__device__ __forceinline__ float xor4(float x) {
  int a = __builtin_amdgcn_update_dpp(0, __float_as_int(x), SHL4, 0xf, 0x5, false);
  a = __builtin_amdgcn_update_dpp(a, __float_as_int(x), SHR4, 0xf, 0xa, false);
  return __int_as_float(a);
}
__device__ __forceinline__ float tr32(float a, float b) {
  auto r = __builtin_amdgcn_permlane32_swap(__float_as_uint(a), __float_as_uint(b), false, false);
  return __uint_as_float(r[0]) + __uint_as_float(r[1]);
}
__device__ __forceinline__ float tr16(float a, float b) {
  auto r = __builtin_amdgcn_permlane16_swap(__float_as_uint(a), __float_as_uint(b), false, false);
  return __uint_as_float(r[0]) + __uint_as_float(r[1]);
}
__device__ __forceinline__ float max32(float a) {
  auto r = __builtin_amdgcn_permlane32_swap(__float_as_uint(a), __float_as_uint(a), false, false);
  return fmaxf(__uint_as_float(r[0]), __uint_as_float(r[1]));
}
__device__ __forceinline__ float max16(float a) {
  auto r = __builtin_amdgcn_permlane16_swap(__float_as_uint(a), __float_as_uint(a), false, false);
  return fmaxf(__uint_as_float(r[0]), __uint_as_float(r[1]));
}
__device__ __forceinline__ float sum32(float a) { return tr32(a, a); }
__device__ __forceinline__ float sum16(float a) { return tr16(a, a); }
__device__ __forceinline__ f32x4 mfma32(v4u a, v4u b, f32x4 c) {
  return __builtin_amdgcn_mfma_f32_16x16x32_bf16(__builtin_bit_cast(bf16x8, a), __builtin_bit_cast(bf16x8, b), c, 0, 0, 0);
}
__device__ __forceinline__ f32x4 mfma16(v2u a, v2u b, f32x4 c) {
  return __builtin_amdgcn_mfma_f32_16x16x16bf16_1k(__builtin_bit_cast(bf16x4, a), __builtin_bit_cast(bf16x4, b), c, 0, 0, 0);
}
template <int N> __device__ __forceinline__ void waitvm() { SB(); asm volatile("s_waitcnt vmcnt(%0)" ::"i"(N) : "memory"); SB(); }
#define STAMP(i)

__device__ __forceinline__ unsigned g_lo(float v, unsigned tl) { return ((__float_as_uint(v) + 0x80u) & 0xffffff00u) | tl; }
__device__ __forceinline__ float g_val(unsigned lo) { return __uint_as_float(lo & 0xffffff00u); }
__device__ __forceinline__ unsigned g_bad(v4u g, unsigned tlo, unsigned thi) {
  return (g.y ^ thi) | (g.w ^ thi) | (((g.x ^ tlo) | (g.z ^ tlo)) & 0xffu);
}
#define LD128(rs, vo, so, aux) __builtin_amdgcn_raw_buffer_load_b128(rs, vo, so, aux)
#define ST128(v, rs, vo, aux) __builtin_amdgcn_raw_buffer_store_b128(v, rs, vo, 0, aux)
#define ST128P(pred, v, rs, vo, aux) __builtin_amdgcn_raw_buffer_store_b128(v, rs, (pred) ? (vo) : 0x7ffffff0, 0, aux)

__global__ void __launch_bounds__(768, 3) fused_decode(Params p) {
  extern __shared__ __attribute__((aligned(16))) unsigned char smem[];
  float* lds = (float*)smem;
  float* L_qred = lds;
  unsigned* L_cnt = (unsigned*)(lds + 256);
  float* L_gate = lds + 264;
  unsigned* L_q = (unsigned*)(lds + 512);
  float* L_m = lds + 768;
  float* L_l = lds + 800;
  float* L_g = lds + 1024;
  float* L_o = lds + 2048;
  STAMP(0);
  const int b = blockIdx.x, tid = threadIdx.x, lane = tid & 63;
  const int wv = __builtin_amdgcn_readfirstlane(tid >> 6);
  const int kvh = b & 7, br = b >> 7, s = (b >> 3) & 15, grp = br * 8 + kvh;
  const __amdgpu_buffer_rsrc_t rW = __builtin_amdgcn_make_buffer_rsrc((void*)p.ws, 0, WS_END, RS_FLAGS);
  const __amdgpu_buffer_rsrc_t rX = __builtin_amdgcn_make_buffer_rsrc((void*)p.x, 0, 1024, RS_FLAGS);
  const int c16 = lane & 15, g4 = lane >> 4;

  if (wv < 4) {
    const int a = wv;
    __builtin_amdgcn_s_setprio(3);
    const unsigned short* wqp = (br ? p.wqB : p.wqA) + (size_t)(s & 1) * 256 * 4096 + kvh * 512 + (s >> 1) * 64;
    const __amdgpu_buffer_rsrc_t rQ = __builtin_amdgcn_make_buffer_rsrc((void*)wqp, 0, 255 * 8192 + 128, RS_FLAGS);
    v4u wq[8];
    {
      const int vo = (64 * a + 8 * (lane >> 3)) * 8192 + (lane & 7) * 16;
#pragma unroll
      for (int i = 0; i < 8; ++i) wq[i] = LD128(rQ, vo, i * 8192, AUX_NT);
    }
    const v4u xf = LD128(rX, ((s & 1) * 256 + 64 * a + 8 * (lane >> 3)) * 2, 0, 0);
    const v2u ep = __builtin_amdgcn_raw_buffer_load_b64(rW, WS_EPOCH, 0, AUX_SC1);
    v4u xa, ga;
    if (a == 3) {
      const __amdgpu_buffer_rsrc_t rG = __builtin_amdgcn_make_buffer_rsrc((void*)p.gw, 0, 1024, RS_FLAGS);
      xa = LD128(rX, lane * 16, 0, 0); ga = LD128(rG, lane * 16, 0, 0);
    }
    v4u pt[3];
    if (PRETOUCH == 1) {
      const int ob = WS_O + (grp * 16 + s) * 16 * MSG;
      pt[0] = LD128(rW, ob + a * 1024 + lane * 16, 0, AUX_SC1);
      pt[1] = LD128(rW, (a < 2 ? ob + (4 + a) * 1024 : WS_Q + (grp * 16 + s) * 512 - 512 * (lane >> 5) + 0) + lane * 16, 0, AUX_SC1);
      pt[2] = LD128(rW, WS_F + b * 4096 + a * 1024 + lane * 16, 0, AUX_SC1);
    }
    if (PRETOUCH == 2 && a < 2) {
      const int off = a == 0 ? (lane < 48 ? WS_O + (grp * 16 + s) * 16 * MSG + lane * 128 : WS_Q + (grp * 16 + s) * 512 + (lane & 3) * 128)
                             : WS_F + b * 4096 + (lane & 31) * 128;
      if (a == 0 ? lane < 52 : lane < 32) asm volatile("global_atomic_add %0, %1, %2 sc1" ::"v"(off), "v"(0u), "s"(p.ws) : "memory");
    }
    SB();
    if (tid < 3) L_cnt[tid] = 0u;
    __syncthreads();
    SB();
    const u64 e64 = (((u64)ep.y << 32) | ep.x) + 1ull;
    const unsigned tlo = (unsigned)e64 & 0xffu, thi = (unsigned)(e64 >> 8);
    float acc[8];
#pragma unroll
    for (int j = 0; j < 8; ++j) acc[j] = 0.f;
#pragma unroll
    for (int k = 0; k < 4; ++k)
#pragma unroll
      for (int j = 0; j < 4; ++j) {
        const unsigned lo = __builtin_amdgcn_perm(wq[2 * k + 1][j], wq[2 * k][j], 0x05040100u);
        const unsigned hi = __builtin_amdgcn_perm(wq[2 * k + 1][j], wq[2 * k][j], 0x07060302u);
        acc[2 * j] = dot2(lo, xf[k], acc[2 * j]);
        acc[2 * j + 1] = dot2(hi, xf[k], acc[2 * j + 1]);
      }
    float a4[4], a2[2];
#pragma unroll
    for (int j = 0; j < 4; ++j) a4[j] = tr32(acc[j], acc[j + 4]);
#pragma unroll
    for (int j = 0; j < 2; ++j) a2[j] = tr16(a4[j], a4[j + 2]);
    const float t0 = a2[0] + dpp<ROR8>(a2[0]), t1 = a2[1] + dpp<ROR8>(a2[1]);
    const float a1 = (lane & 8) ? t1 : t0;
    const int col = 8 * (lane & 7) + ((lane >> 3) & 1) + 2 * ((lane >> 4) & 1) + 4 * (lane >> 5);
    L_qred[a * 64 + col] = a1;
    STAMP(1);
    unsigned old = 0;
    asm volatile("s_waitcnt lgkmcnt(0)" ::: "memory");
    if (lane == 0) old = __hip_atomic_fetch_add(L_cnt, 1u, __ATOMIC_RELAXED, __HIP_MEMORY_SCOPE_WORKGROUP);
    old = __builtin_amdgcn_readfirstlane(old);
    if (old == 3u) {
      asm volatile("" ::: "memory");
      float q0 = 0.f, q1 = 0.f;
#pragma unroll
      for (int ww = 0; ww < 2; ++ww) { const f32x2 t = *(const f32x2*)(L_qred + (2 * (lane >> 5) + ww) * 64 + 2 * (lane & 31)); q0 += t.x; q1 += t.y; }
      q0 = sum32(q0); q1 = sum32(q1);
      const v4u g = {g_lo(q0, tlo), thi, g_lo(q1, tlo), thi};
      if (lane < 32) ST128(g, rW, WS_Q + (grp * 16 + s) * 512 + lane * 16, AUX_ST);
      asm volatile("s_waitcnt vmcnt(0)" ::: "memory");
      if (lane == 0) __hip_atomic_store(L_cnt + 2, 1u, __ATOMIC_RELAXED, __HIP_MEMORY_SCOPE_WORKGROUP);
    }
    if (a == 3) {
      float gs = 0.f;
#pragma unroll
      for (int j = 0; j < 4; ++j) {
        const unsigned pr = cvtpk(bflo(xa[j]) * bflo(ga[j]), bfhi(xa[j]) * bfhi(ga[j]));
        gs += bflo(pr) + bfhi(pr);
      }
      gs += dpp<XOR1>(gs); gs += dpp<XOR2>(gs); gs += dpp<HMIR>(gs); gs += dpp<MIR>(gs);
      gs = sum16(gs); gs = sum32(gs);
      const float gate = rbf(__builtin_amdgcn_rcpf(1.f + __expf(-rbf(gs))));
      if (lane == 0) L_gate[0] = gate;
    }
    STAMP(2);
    const int qoff = WS_Q + (grp * 16 + 4 * a + 2 * (lane >> 5)) * 512 + (lane & 31) * 16;
    while (__builtin_amdgcn_readfirstlane(__hip_atomic_load(L_cnt + 2, __ATOMIC_RELAXED, __HIP_MEMORY_SCOPE_WORKGROUP)) == 0u) __builtin_amdgcn_s_sleep(1);
    __builtin_amdgcn_s_sleep(QD1);
    SB();
    v4u qa = LD128(rW, qoff, 0, AUX_SC1), qb = LD128(rW, qoff, 512, AUX_SC1);
    SB();
    __builtin_amdgcn_s_sleep(QD2);
    SB();
    v4u qc = LD128(rW, qoff, 0, AUX_SC1), qd = LD128(rW, qoff, 512, AUX_SC1);
    SB();
    STAMP(3);
    if (!__all((g_bad(qa, tlo, thi) | g_bad(qb, tlo, thi)) == 0u)) {
      if (g_bad(qa, tlo, thi)) qa = qc;
      if (g_bad(qb, tlo, thi)) qb = qd;
      for (unsigned spins = 0; spins < SPIN_CAP; ++spins) {
        const bool ba = g_bad(qa, tlo, thi) != 0u, bb = g_bad(qb, tlo, thi) != 0u;
        if (!__any(ba | bb)) break;
        if (ba) qa = LD128(rW, qoff, 0, AUX_SC1);
        if (bb) qb = LD128(rW, qoff, 512, AUX_SC1);
        __builtin_amdgcn_s_sleep(2);
      }
    }
    L_q[a * 64 + lane] = cvtpk(g_val(qa.x) + g_val(qb.x), g_val(qa.z) + g_val(qb.z));
    asm volatile("s_waitcnt lgkmcnt(0)" ::: "memory");
    if (lane == 0) __hip_atomic_fetch_add(L_cnt + 1, 1u, __ATOMIC_RELAXED, __HIP_MEMORY_SCOPE_WORKGROUP);
    if (PRETOUCH == 1) asm volatile("" ::"v"(pt[0]), "v"(pt[1]), "v"(pt[2]));
    if (PRETOUCH == 3 && a < 2) {
      const int off = a == 0 ? (lane < 48 ? WS_O + (grp * 16 + s) * 16 * MSG + lane * 128 : WS_EPOCH) : WS_F + b * 4096 + (lane & 31) * 128;
      const unsigned t = __builtin_amdgcn_raw_buffer_load_b32(rW, off, 0, AUX_SC1);
      asm volatile("" ::"v"(t));
    }
    STAMP(4);
    return;
  }
  const int w = wv - 4;
  const unsigned short* kp = (br ? p.kB : p.kA) + ((size_t)kvh * SK + (SK - WIN) + s * 256) * DH;
  const unsigned short* vp = (br ? p.vB : p.vA) + ((size_t)kvh * SK + (SK - WIN) + s * 256) * DH;
  const unsigned short* wop = (br ? p.woB : p.woA) + (size_t)(kvh * 512 + s * 32) * 512;
  const __amdgpu_buffer_rsrc_t rK = __builtin_amdgcn_make_buffer_rsrc((void*)kp, 0, 65536, RS_FLAGS);
  const __amdgpu_buffer_rsrc_t rV = __builtin_amdgcn_make_buffer_rsrc((void*)vp, 0, 65536, RS_FLAGS);
  const __amdgpu_buffer_rsrc_t rO = __builtin_amdgcn_make_buffer_rsrc((void*)wop, 0, 32768, RS_FLAGS);
  v4u kf[8], vf[8], wo[4];
  const int kvo = w * 8192 + (c16 >> 2) * 256 + g4 * 64 + (lane & 3) * 16;
  const int vvo = w * 8192 + lane * 16;
  const unsigned rot = (((unsigned)(b * 8 + w) * 2654435761u) >> 16);
#define KVOFF(i) ((int)(((i) + rot) & 7u) * 1024)
  if (PRIO) __builtin_amdgcn_s_setprio(2);
  const v2u ep = __builtin_amdgcn_raw_buffer_load_b64(rW, WS_EPOCH, 0, AUX_SC1);
  SB();
  __syncthreads();
  SB();
  if (KDELAY) __builtin_amdgcn_s_sleep(KDELAY);
  SB();
#pragma unroll
  for (int i = 0; i < 8; ++i) { if (i >= WND) waitvm<WND - 1>(); kf[i] = LD128(rK, kvo, KVOFF(i), AUX_NT); SB(); }
  const int wvo = (8 * (w & 3) + 4 * (w >> 2)) * 1024 + lane * 16;
#pragma unroll
  for (int j = 0; j < WOB; ++j) { waitvm<WND - 1>(); wo[j] = LD128(rO, wvo, j * 1024, AUX_NT); SB(); }
  STAMP(2);
  if (PRIO) __builtin_amdgcn_s_setprio(1);
#define QFLAG() (__builtin_amdgcn_readfirstlane(__hip_atomic_load(L_cnt + 1, __ATOMIC_RELAXED, __HIP_MEMORY_SCOPE_WORKGROUP)) == 4u)
  bool haveq = false;
#pragma unroll
  for (int i = 0; i < 8; ++i) {
    if (!haveq) { waitvm<WND - 1>(); haveq = QFLAG(); }
    SB();
    vf[i] = LD128(rV, vvo, KVOFF(i), AUX_NT);
    SB();
  }
  STAMP(3);
  if (PRIO) __builtin_amdgcn_s_setprio(2);
  const u64 e64 = (((u64)ep.y << 32) | ep.x) + 1ull;
  const unsigned tlo = (unsigned)e64 & 0xffu, thi = (unsigned)(e64 >> 8);
  while (!haveq) { haveq = QFLAG(); if (!haveq) __builtin_amdgcn_s_sleep(1); }
  asm volatile("" ::: "memory");
  STAMP(4);
  const v4u qf = *(const v4u*)(L_q + (c16 >> 2) * 64 + 4 * (4 * g4 + (c16 & 3)));
  float sc[8];
  {
    f32x4 d[8];
#pragma unroll
    for (int i = 0; i < 8; ++i) d[i] = mfma32(kf[i], qf, f32x4{0.f, 0.f, 0.f, 0.f});
#pragma unroll
    for (int i = 0; i < 8; ++i) sc[i] = (dpp<0x00>(d[i][0]) + dpp<0x55>(d[i][1])) + (dpp<0xAA>(d[i][2]) + dpp<0xFF>(d[i][3]));
  }
#pragma unroll
  for (int i = 0; i < 8; i += 2) {
    const unsigned r1 = cvtpk(sc[i], sc[i + 1]);
    const unsigned r2 = cvtpk(bflo(r1) * 0.08838834764831845f, bfhi(r1) * 0.08838834764831845f);
    sc[i] = bflo(r2); sc[i + 1] = bfhi(r2);
  }
  constexpr float LOG2E = 1.4426950408889634f;
  float m = fmaxf(fmaxf(fmaxf(sc[0], sc[1]), fmaxf(sc[2], sc[3])), fmaxf(fmaxf(sc[4], sc[5]), fmaxf(sc[6], sc[7])));
  m = max16(m); m = max32(m);
  float pe[8];
  float l = 0.f;
  {
    const float mb = m * LOG2E;
#pragma unroll
    for (int i = 0; i < 8; ++i) { pe[i] = __builtin_amdgcn_exp2f(sc[i] * LOG2E - mb); l += pe[i]; }
    l = sum16(l); l = sum32(l);
  }
  if (lane < 16 && !(lane & 3)) { L_m[(lane >> 2) * 8 + w] = m; L_l[(lane >> 2) * 8 + w] = l; }
  STAMP(5);
  __syncthreads();
  unsigned pk[4];
  {
    const f32x4 m0 = *(const f32x4*)(L_m + (c16 >> 2) * 8), m1 = *(const f32x4*)(L_m + (c16 >> 2) * 8 + 4);
    const float M = fmaxf(fmaxf(fmaxf(m0[0], m0[1]), fmaxf(m0[2], m0[3])), fmaxf(fmaxf(m1[0], m1[1]), fmaxf(m1[2], m1[3])));
    const float scale = __builtin_amdgcn_exp2f((m - M) * LOG2E);
#pragma unroll
    for (int i = 0; i < 4; ++i) pk[i] = cvtpk(pe[2 * i] * scale, pe[2 * i + 1] * scale);
  }
  {
    const int h = w >> 1;
    const float mw = L_m[h * 8 + (lane & 7)], lw = L_l[h * 8 + (lane & 7)];
    float M = fmaxf(mw, dpp<XOR1>(mw)); M = fmaxf(M, dpp<XOR2>(M)); M = fmaxf(M, dpp<HMIR>(M));
    float L = lw * __builtin_amdgcn_exp2f((mw - M) * LOG2E);
    L += dpp<XOR1>(L); L += dpp<XOR2>(L); L += dpp<HMIR>(L);
    const v4u gs = {g_lo(M, tlo), thi, g_lo(L, tlo), thi};
    ST128P(lane < 2, gs, rW, WS_O + ((grp * 16 + 2 * w + lane) * 16 + s) * MSG + 256, AUX_ST);
  }
  SB();
  STAMP(6);
  unsigned selE[2], selO[2];
  {
    const int jp = lane & 3;
#pragma unroll
    for (int r = 0; r < 2; ++r) {
      const bool on = (jp >> 1) == r;
      selE[r] = on ? ((jp & 1) ? 0x01000c0cu : 0x0c0c0100u) : 0x0c0c0c0cu;
      selO[r] = on ? ((jp & 1) ? 0x03020c0cu : 0x0c0c0302u) : 0x0c0c0c0cu;
    }
  }
  f32x4 acc0 = {0.f, 0.f, 0.f, 0.f}, acc1 = {0.f, 0.f, 0.f, 0.f};
#pragma unroll
  for (int i = 0; i < 8; ++i) {
    v2u a;
    a.x = __builtin_amdgcn_perm(pk[i >> 1], pk[i >> 1], (i & 1) ? selO[0] : selE[0]);
    a.y = __builtin_amdgcn_perm(pk[i >> 1], pk[i >> 1], (i & 1) ? selO[1] : selE[1]);
    acc0 = mfma16(a, v2u{vf[i].x, vf[i].y}, acc0);
    acc1 = mfma16(a, v2u{vf[i].z, vf[i].w}, acc1);
  }
  *(f32x4*)(L_o + w * 512 + g4 * 128 + 8 * c16) = acc0;
  *(f32x4*)(L_o + w * 512 + g4 * 128 + 8 * c16 + 4) = acc1;
  STAMP(7);
  __syncthreads();
  {
    float o0 = 0.f, o1 = 0.f;
#pragma unroll
    for (int ww = 0; ww < 4; ++ww) { const f32x2 t = *(const f32x2*)(L_o + (4 * (lane >> 5) + ww) * 512 + w * 64 + 2 * (lane & 31)); o0 += t.x; o1 += t.y; }
    o0 = sum32(o0); o1 = sum32(o1);
    const v4u g = {g_lo(o0, tlo), thi, g_lo(o1, tlo), thi};
    ST128P(lane < 32, g, rW, WS_O + ((grp * 16 + 2 * w + (lane >> 4)) * 16 + s) * MSG + (lane & 15) * 16, AUX_ST);
  }
  SB();
  STAMP(8);
#pragma unroll
  for (int j = WOB; j < 4; ++j) wo[j] = LD128(rO, wvo, j * 1024, AUX_NT);
  SB();
  {
    if (OACK) waitvm<4 - WOB>();
    if (ODELAY) __builtin_amdgcn_s_sleep(ODELAY);
    SB();
    STAMP(9);
    const int ooff = WS_O + ((grp * 16 + s) * 16 + (lane >> 2)) * MSG;
    const int doff = ooff + (w & 3) * 64 + (lane & 3) * 16;
    v4u od = LD128(rW, doff, 0, AUX_SC1);
    v4u sg = LD128(rW, ooff + 256, 0, AUX_SC1);
    SB();
    unsigned wl[8], wh[8];
#pragma unroll
    for (int k = 0; k < 2; ++k)
#pragma unroll
      for (int j = 0; j < 4; ++j) {
        wl[k * 4 + j] = __builtin_amdgcn_perm(wo[2 * k + 1][j], wo[2 * k][j], 0x05040100u);
        wh[k * 4 + j] = __builtin_amdgcn_perm(wo[2 * k + 1][j], wo[2 * k][j], 0x07060302u);
        asm volatile("" : "+v"(wl[k * 4 + j]), "+v"(wh[k * 4 + j]));
      }
    SB();
    for (unsigned spins = 0; spins < SPIN_CAP; ++spins) {
      const bool b0 = g_bad(od, tlo, thi) != 0u, b2 = g_bad(sg, tlo, thi) != 0u;
      if (!__any(b0 | b2)) break;
      __builtin_amdgcn_s_sleep(OSLEEP);
      if (b0) od = LD128(rW, doff, 0, AUX_SC1);
      if (b2) sg = LD128(rW, ooff + 256, 0, AUX_SC1);
    }
    STAMP(10);
    const float ms = g_val(sg.x), ls = g_val(sg.z);
    float M = fmaxf(ms, xor4(ms)); M = fmaxf(M, dpp<ROR8>(M)); M = max16(M); M = max32(M);
    const float wgt = __builtin_amdgcn_exp2f((ms - M) * LOG2E);
    float L = wgt * ls;
    float ov = tr32(wgt * g_val(od.x), wgt * g_val(od.z));
    L += xor4(L); ov += xor4(ov);
    L += dpp<ROR8>(L); ov += dpp<ROR8>(ov);
    L = sum16(L); ov = sum16(ov);
    L = sum32(L);
    ov *= __builtin_amdgcn_rcpf(L);
    const unsigned pd = cvtpk(ov, ov);
    const int l0 = 2 * (w >> 2);
    const unsigned p0 = __builtin_amdgcn_readlane(pd, l0), p1 = __builtin_amdgcn_readlane(pd, l0 + 32), p2 = __builtin_amdgcn_readlane(pd, l0 + 1), p3 = __builtin_amdgcn_readlane(pd, l0 + 33);
    const unsigned q01 = (p0 & 0xffffu) | (p1 & 0xffff0000u), q23 = (p2 & 0xffffu) | (p3 & 0xffff0000u);
    f32x4 f0, f1;
#pragma unroll
    for (int j = 0; j < 4; ++j) {
      const float lo = dot2(wl[4 + j], q23, dot2(wl[j], q01, 0.f)), hi = dot2(wh[4 + j], q23, dot2(wh[j], q01, 0.f));
      if (j < 2) { f0[2 * j] = lo; f0[2 * j + 1] = hi; } else { f1[2 * j - 4] = lo; f1[2 * j - 3] = hi; }
    }
    *(f32x4*)(L_o + w * 512 + 8 * lane) = f0;
    *(f32x4*)(L_o + w * 512 + 8 * lane + 4) = f1;
  }
  __syncthreads();
  {
    float f0 = 0.f, f1 = 0.f;
#pragma unroll
    for (int ww = 0; ww < 4; ++ww) { const f32x2 t = *(const f32x2*)(L_o + (4 * (lane >> 5) + ww) * 512 + w * 64 + 2 * (lane & 31)); f0 += t.x; f1 += t.y; }
    f0 = sum32(f0); f1 = sum32(f1);
    const v4u g = {g_lo(f0, tlo), thi, g_lo(f1, tlo), thi};
    ST128P(lane < 32, g, rW, WS_F + b * 4096 + (w * 64 + 2 * lane) * 8, AUX_ST);
  }
  STAMP(11);
  if (b < 32) {
    if (FACK) waitvm<0>();
    if (FDELAY) __builtin_amdgcn_s_sleep(FDELAY);
    SB();
    const int foff = WS_F + (32 * w + (lane >> 3)) * 4096 + b * 128 + (lane & 7) * 16;
    v4u fd[4];
#pragma unroll
    for (int i = 0; i < 4; ++i) fd[i] = LD128(rW, foff, i * 8 * 4096, AUX_SC1);
    for (unsigned spins = 0; spins < SPIN_CAP; ++spins) {
      bool bd[4];
#pragma unroll
      for (int i = 0; i < 4; ++i) bd[i] = g_bad(fd[i], tlo, thi) != 0u;
      if (!__any(bd[0] | bd[1] | bd[2] | bd[3])) break;
#pragma unroll
      for (int i = 0; i < 4; ++i) if (bd[i]) fd[i] = LD128(rW, foff, i * 8 * 4096, AUX_SC1);
      __builtin_amdgcn_s_sleep(1);
    }
    STAMP(12);
    float s0 = (g_val(fd[0].x) + g_val(fd[1].x)) + (g_val(fd[2].x) + g_val(fd[3].x));
    float s1 = (g_val(fd[0].z) + g_val(fd[1].z)) + (g_val(fd[2].z) + g_val(fd[3].z));
    s0 += dpp<ROR8>(s0); s1 += dpp<ROR8>(s1);
    const float t = sum32(tr16(s0, s1));
    if (lane < 8 || (lane >= 16 && lane < 24)) L_g[w * 16 + 2 * (lane & 7) + (lane >> 4)] = t;
    __syncthreads();
    if (w == 0) {
      const int c = lane & 15;
      const float a = (L_g[c] + L_g[16 + c]) + (L_g[32 + c] + L_g[48 + c]), bb = (L_g[64 + c] + L_g[80 + c]) + (L_g[96 + c] + L_g[112 + c]);
      const float gate = L_gate[0], omg = rbf(1.f - gate);
      const float r = rbf(gate * rbf(a)) + rbf(omg * rbf(bb));
      const float rn = dpp<XOR1>(r);
      if (lane < 16 && !(lane & 1)) ((unsigned*)p.out)[b * 8 + (lane >> 1)] = cvtpk(r, rn);
      if (b == 0 && lane == 0) *(u64*)(p.ws + WS_EPOCH) = e64;
    }
    STAMP(13);
  }
}

extern "C" void kernel_launch(void* const* d_in, const int* in_sizes, int n_in, void* d_out, int out_size, void* d_ws, size_t ws_size, hipStream_t stream) {
  Params p;
  p.x = (const unsigned short*)d_in[0]; p.kA = (const unsigned short*)d_in[1]; p.vA = (const unsigned short*)d_in[2];
  p.wqA = (const unsigned short*)d_in[3]; p.woA = (const unsigned short*)d_in[4];
  p.kB = (const unsigned short*)d_in[6]; p.vB = (const unsigned short*)d_in[7];
  p.wqB = (const unsigned short*)d_in[8]; p.woB = (const unsigned short*)d_in[9]; p.gw = (const unsigned short*)d_in[10];
  p.out = (unsigned short*)d_out; p.ws = (unsigned char*)d_ws + ws_off(ws_size);
  (void)hipFuncSetAttribute((const void*)fused_decode, hipFuncAttributeMaxDynamicSharedMemorySize, LDS_BYTES);
  fused_decode<<<256, 768, LDS_BYTES, stream>>>(p);
}
```

```cpp
#define PROBE 0
#include <hip/hip_runtime.h>
#include <stdint.h>

typedef unsigned v4u __attribute__((ext_vector_type(4)));
typedef unsigned v2u __attribute__((ext_vector_type(2)));
typedef float f32x4 __attribute__((ext_vector_type(4)));
typedef float f32x2 __attribute__((ext_vector_type(2)));
typedef short bf16x8 __attribute__((ext_vector_type(8)));
typedef short bf16x4 __attribute__((ext_vector_type(4)));
typedef __bf16 bf16x2 __attribute__((ext_vector_type(2)));
typedef unsigned long long u64;
#define SB() __builtin_amdgcn_sched_barrier(0)

constexpr int SK = 32768, WIN = 4096, DH = 128;
constexpr int AUX_NT = 2, RS_FLAGS = 0x00020000;
#define HLD 16
#define HST 16
constexpr int AUX_SC1 = HLD, AUX_ST = HST;
constexpr int WS_EPOCH = 0;
constexpr int WS_Q = 4096;
constexpr int MSG = 384;
constexpr int WS_O = WS_Q + 16 * 16 * 512;
constexpr int WS_F = WS_O + 16 * 16 * 16 * MSG;
constexpr int WS_END = WS_F + 256 * 512 * 8;
constexpr int WS_STAMP = 4 << 20;
constexpr unsigned SPIN_CAP = 1u << 14;
constexpr int LDS_BYTES = 96 * 1024;
#define WOB 4
#define ODELAY 4
#define FDELAY 4
#define OACK 0
#define OSLEEP 2
#define FACK 0
#define PRETOUCH 0
#define WND 3
#define KDELAY 0
#define QD1 8
#define QD2 20
#define PRIO 1
#define OWAIT 2

#define WSEND 1
static inline size_t ws_off(size_t ws_size) { return WSEND ? ((ws_size - (size_t)(WSEND) * (8u << 20)) & ~(size_t)0xfffff) : 0; }
struct Params {
  const unsigned short *x, *kA, *vA, *wqA, *woA, *kB, *vB, *wqB, *woB, *gw;
  unsigned short* out;
  unsigned char* ws;
};

__device__ __forceinline__ float dot2(unsigned a, unsigned b, float c) {
  return __builtin_amdgcn_fdot2_f32_bf16(__builtin_bit_cast(bf16x2, a), __builtin_bit_cast(bf16x2, b), c, false);
}
__device__ __forceinline__ unsigned cvtpk(float lo, float hi) {
  bf16x2 r = {(__bf16)lo, (__bf16)hi};
  return __builtin_bit_cast(unsigned, r);
}
__device__ __forceinline__ float bflo(unsigned u) { return __uint_as_float(u << 16); }
__device__ __forceinline__ float bfhi(unsigned u) { return __uint_as_float(u & 0xffff0000u); }
__device__ __forceinline__ float rbf(float x) { return bflo(cvtpk(x, x)); }
template <int CTRL> __device__ __forceinline__ float dpp(float x) {
  return __int_as_float(__builtin_amdgcn_mov_dpp(__float_as_int(x), CTRL, 0xf, 0xf, true));
}
template <int CTRL> __device__ __forceinline__ unsigned dppu(unsigned x) {
  return (unsigned)__builtin_amdgcn_mov_dpp((int)x, CTRL, 0xf, 0xf, true);
}
constexpr int XOR1 = 0xB1, XOR2 = 0x4E, HMIR = 0x141, MIR = 0x140, ROR8 = 0x128, SHL4 = 0x104, SHR4 = 0x114;
__device__ __forceinline__ float xor4(float x) {
  int a = __builtin_amdgcn_update_dpp(0, __float_as_int(x), SHL4, 0xf, 0x5, false);
  a = __builtin_amdgcn_update_dpp(a, __float_as_int(x), SHR4, 0xf, 0xa, false);
  return __int_as_float(a);
}
__device__ __forceinline__ float tr32(float a, float b) {
  auto r = __builtin_amdgcn_permlane32_swap(__float_as_uint(a), __float_as_uint(b), false, false);
  return __uint_as_float(r[0]) + __uint_as_float(r[1]);
}
__device__ __forceinline__ float tr16(float a, float b) {
  auto r = __builtin_amdgcn_permlane16_swap(__float_as_uint(a), __float_as_uint(b), false, false);
  return __uint_as_float(r[0]) + __uint_as_float(r[1]);
}
__device__ __forceinline__ float max32(float a) {
  auto r = __builtin_amdgcn_permlane32_swap(__float_as_uint(a), __float_as_uint(a), false, false);
  return fmaxf(__uint_as_float(r[0]), __uint_as_float(r[1]));
}
__device__ __forceinline__ float max16(float a) {
  auto r = __builtin_amdgcn_permlane16_swap(__float_as_uint(a), __float_as_uint(a), false, false);
  return fmaxf(__uint_as_float(r[0]), __uint_as_float(r[1]));
}
__device__ __forceinline__ float sum32(float a) { return tr32(a, a); }
__device__ __forceinline__ float sum16(float a) { return tr16(a, a); }
__device__ __forceinline__ f32x4 mfma32(v4u a, v4u b, f32x4 c) {
  return __builtin_amdgcn_mfma_f32_16x16x32_bf16(__builtin_bit_cast(bf16x8, a), __builtin_bit_cast(bf16x8, b), c, 0, 0, 0);
}
__device__ __forceinline__ f32x4 mfma16(v2u a, v2u b, f32x4 c) {
  return __builtin_amdgcn_mfma_f32_16x16x16bf16_1k(__builtin_bit_cast(bf16x4, a), __builtin_bit_cast(bf16x4, b), c, 0, 0, 0);
}
template <int N> __device__ __forceinline__ void waitvm() { SB(); asm volatile("s_waitcnt vmcnt(%0)" ::"i"(N) : "memory"); SB(); }
__device__ __forceinline__ u64 rt() { u64 t; asm volatile("s_memrealtime %0\n s_waitcnt lgkmcnt(0)" : "=s"(t)::"memory"); return t; }
#define STAMP(i) do { if ((i) == PROBE) { SB(); tprobe = rt(); SB(); } } while (0)

__device__ __forceinline__ unsigned g_lo(float v, unsigned tl) { return ((__float_as_uint(v) + 0x80u) & 0xffffff00u) | tl; }
__device__ __forceinline__ float g_val(unsigned lo) { return __uint_as_float(lo & 0xffffff00u); }
__device__ __forceinline__ unsigned g_bad(v4u g, unsigned tlo, unsigned thi) {
  return (g.y ^ thi) | (g.w ^ thi) | (((g.x ^ tlo) | (g.z ^ tlo)) & 0xffu);
}
#define LD128(rs, vo, so, aux) __builtin_amdgcn_raw_buffer_load_b128(rs, vo, so, aux)
#define ST128(v, rs, vo, aux) __builtin_amdgcn_raw_buffer_store_b128(v, rs, vo, 0, aux)
#define ST128P(pred, v, rs, vo, aux) __builtin_amdgcn_raw_buffer_store_b128(v, rs, (pred) ? (vo) : 0x7ffffff0, 0, aux)

__global__ void __launch_bounds__(768, 3) fused_decode(Params p) {
  extern __shared__ __attribute__((aligned(16))) unsigned char smem[];
  float* lds = (float*)smem;
  float* L_qred = lds;
  unsigned* L_cnt = (unsigned*)(lds + 256);
  float* L_gate = lds + 264;
  unsigned* L_q = (unsigned*)(lds + 512);
  float* L_m = lds + 768;
  float* L_l = lds + 800;
  float* L_g = lds + 1024;
  float* L_o = lds + 2048;
  u64 tprobe = 0;
  STAMP(0);
  const int b = blockIdx.x, tid = threadIdx.x, lane = tid & 63;
  const int wv = __builtin_amdgcn_readfirstlane(tid >> 6);
  const int kvh = b & 7, br = b >> 7, s = (b >> 3) & 15, grp = br * 8 + kvh;
  const __amdgpu_buffer_rsrc_t rW = __builtin_amdgcn_make_buffer_rsrc((void*)p.ws, 0, WS_END, RS_FLAGS);
  const __amdgpu_buffer_rsrc_t rX = __builtin_amdgcn_make_buffer_rsrc((void*)p.x, 0, 1024, RS_FLAGS);
  const int c16 = lane & 15, g4 = lane >> 4;

  if (wv < 4) {
    const int a = wv;
    __builtin_amdgcn_s_setprio(3);
    const unsigned short* wqp = (br ? p.wqB : p.wqA) + (size_t)(s & 1) * 256 * 4096 + kvh * 512 + (s >> 1) * 64;
    const __amdgpu_buffer_rsrc_t rQ = __builtin_amdgcn_make_buffer_rsrc((void*)wqp, 0, 255 * 8192 + 128, RS_FLAGS);
    v4u wq[8];
    {
      const int vo = (64 * a + 8 * (lane >> 3)) * 8192 + (lane & 7) * 16;
#pragma unroll
      for (int i = 0; i < 8; ++i) wq[i] = LD128(rQ, vo, i * 8192, AUX_NT);
    }
    const v4u xf = LD128(rX, ((s & 1) * 256 + 64 * a + 8 * (lane >> 3)) * 2, 0, 0);
    const v2u ep = __builtin_amdgcn_raw_buffer_load_b64(rW, WS_EPOCH, 0, AUX_SC1);
    v4u xa, ga;
    if (a == 3) {
      const __amdgpu_buffer_rsrc_t rG = __builtin_amdgcn_make_buffer_rsrc((void*)p.gw, 0, 1024, RS_FLAGS);
      xa = LD128(rX, lane * 16, 0, 0); ga = LD128(rG, lane * 16, 0, 0);
    }
    v4u pt[3];
    if (PRETOUCH == 1) {
      const int ob = WS_O + (grp * 16 + s) * 16 * MSG;
      pt[0] = LD128(rW, ob + a * 1024 + lane * 16, 0, AUX_SC1);
      pt[1] = LD128(rW, (a < 2 ? ob + (4 + a) * 1024 : WS_Q + (grp * 16 + s) * 512 - 512 * (lane >> 5) + 0) + lane * 16, 0, AUX_SC1);
      pt[2] = LD128(rW, WS_F + b * 4096 + a * 1024 + lane * 16, 0, AUX_SC1);
    }
    if (PRETOUCH == 2 && a < 2) {
      const int off = a == 0 ? (lane < 48 ? WS_O + (grp * 16 + s) * 16 * MSG + lane * 128 : WS_Q + (grp * 16 + s) * 512 + (lane & 3) * 128)
                             : WS_F + b * 4096 + (lane & 31) * 128;
      if (a == 0 ? lane < 52 : lane < 32) asm volatile("global_atomic_add %0, %1, %2 sc1" ::"v"(off), "v"(0u), "s"(p.ws) : "memory");
    }
    SB();
    if (tid < 3) L_cnt[tid] = 0u;
    __syncthreads();
    SB();
    const u64 e64 = (((u64)ep.y << 32) | ep.x) + 1ull;
    const unsigned tlo = (unsigned)e64 & 0xffu, thi = (unsigned)(e64 >> 8);
    float acc[8];
#pragma unroll
    for (int j = 0; j < 8; ++j) acc[j] = 0.f;
#pragma unroll
    for (int k = 0; k < 4; ++k)
#pragma unroll
      for (int j = 0; j < 4; ++j) {
        const unsigned lo = __builtin_amdgcn_perm(wq[2 * k + 1][j], wq[2 * k][j], 0x05040100u);
        const unsigned hi = __builtin_amdgcn_perm(wq[2 * k + 1][j], wq[2 * k][j], 0x07060302u);
        acc[2 * j] = dot2(lo, xf[k], acc[2 * j]);
        acc[2 * j + 1] = dot2(hi, xf[k], acc[2 * j + 1]);
      }
    float a4[4], a2[2];
#pragma unroll
    for (int j = 0; j < 4; ++j) a4[j] = tr32(acc[j], acc[j + 4]);
#pragma unroll
    for (int j = 0; j < 2; ++j) a2[j] = tr16(a4[j], a4[j + 2]);
    const float t0 = a2[0] + dpp<ROR8>(a2[0]), t1 = a2[1] + dpp<ROR8>(a2[1]);
    const float a1 = (lane & 8) ? t1 : t0;
    const int col = 8 * (lane & 7) + ((lane >> 3) & 1) + 2 * ((lane >> 4) & 1) + 4 * (lane >> 5);
    L_qred[a * 64 + col] = a1;
    STAMP(1);
    unsigned old = 0;
    asm volatile("s_waitcnt lgkmcnt(0)" ::: "memory");
    if (lane == 0) old = __hip_atomic_fetch_add(L_cnt, 1u, __ATOMIC_RELAXED, __HIP_MEMORY_SCOPE_WORKGROUP);
    old = __builtin_amdgcn_readfirstlane(old);
    if (old == 3u) {
      asm volatile("" ::: "memory");
      float q0 = 0.f, q1 = 0.f;
#pragma unroll
      for (int ww = 0; ww < 2; ++ww) { const f32x2 t = *(const f32x2*)(L_qred + (2 * (lane >> 5) + ww) * 64 + 2 * (lane & 31)); q0 += t.x; q1 += t.y; }
      q0 = sum32(q0); q1 = sum32(q1);
      const v4u g = {g_lo(q0, tlo), thi, g_lo(q1, tlo), thi};
      if (lane < 32) ST128(g, rW, WS_Q + (grp * 16 + s) * 512 + lane * 16, AUX_ST);
      asm volatile("s_waitcnt vmcnt(0)" ::: "memory");
      if (lane == 0) __hip_atomic_store(L_cnt + 2, 1u, __ATOMIC_RELAXED, __HIP_MEMORY_SCOPE_WORKGROUP);
    }
    if (a == 3) {
      float gs = 0.f;
#pragma unroll
      for (int j = 0; j < 4; ++j) {
        const unsigned pr = cvtpk(bflo(xa[j]) * bflo(ga[j]), bfhi(xa[j]) * bfhi(ga[j]));
        gs += bflo(pr) + bfhi(pr);
      }
      gs += dpp<XOR1>(gs); gs += dpp<XOR2>(gs); gs += dpp<HMIR>(gs); gs += dpp<MIR>(gs);
      gs = sum16(gs); gs = sum32(gs);
      const float gate = rbf(__builtin_amdgcn_rcpf(1.f + __expf(-rbf(gs))));
      if (lane == 0) L_gate[0] = gate;
    }
    STAMP(2);
    const int qoff = WS_Q + (grp * 16 + 4 * a + 2 * (lane >> 5)) * 512 + (lane & 31) * 16;
    while (__builtin_amdgcn_readfirstlane(__hip_atomic_load(L_cnt + 2, __ATOMIC_RELAXED, __HIP_MEMORY_SCOPE_WORKGROUP)) == 0u) __builtin_amdgcn_s_sleep(1);
    __builtin_amdgcn_s_sleep(QD1);
    SB();
    v4u qa = LD128(rW, qoff, 0, AUX_SC1), qb = LD128(rW, qoff, 512, AUX_SC1);
    SB();
    __builtin_amdgcn_s_sleep(QD2);
    SB();
    v4u qc = LD128(rW, qoff, 0, AUX_SC1), qd = LD128(rW, qoff, 512, AUX_SC1);
    SB();
    STAMP(3);
    if (!__all((g_bad(qa, tlo, thi) | g_bad(qb, tlo, thi)) == 0u)) {
      if (g_bad(qa, tlo, thi)) qa = qc;
      if (g_bad(qb, tlo, thi)) qb = qd;
      for (unsigned spins = 0; spins < SPIN_CAP; ++spins) {
        const bool ba = g_bad(qa, tlo, thi) != 0u, bb = g_bad(qb, tlo, thi) != 0u;
        if (!__any(ba | bb)) break;
        if (ba) qa = LD128(rW, qoff, 0, AUX_SC1);
        if (bb) qb = LD128(rW, qoff, 512, AUX_SC1);
        __builtin_amdgcn_s_sleep(2);
      }
    }
    L_q[a * 64 + lane] = cvtpk(g_val(qa.x) + g_val(qb.x), g_val(qa.z) + g_val(qb.z));
    asm volatile("s_waitcnt lgkmcnt(0)" ::: "memory");
    if (lane == 0) __hip_atomic_fetch_add(L_cnt + 1, 1u, __ATOMIC_RELAXED, __HIP_MEMORY_SCOPE_WORKGROUP);
    if (PRETOUCH == 1) asm volatile("" ::"v"(pt[0]), "v"(pt[1]), "v"(pt[2]));
    if (PRETOUCH == 3 && a < 2) {
      const int off = a == 0 ? (lane < 48 ? WS_O + (grp * 16 + s) * 16 * MSG + lane * 128 : WS_EPOCH) : WS_F + b * 4096 + (lane & 31) * 128;
      const unsigned t = __builtin_amdgcn_raw_buffer_load_b32(rW, off, 0, AUX_SC1);
      asm volatile("" ::"v"(t));
    }
    STAMP(4);
    return;
  }
  const int w = wv - 4;
  const unsigned short* kp = (br ? p.kB : p.kA) + ((size_t)kvh * SK + (SK - WIN) + s * 256) * DH;
  const unsigned short* vp = (br ? p.vB : p.vA) + ((size_t)kvh * SK + (SK - WIN) + s * 256) * DH;
  const unsigned short* wop = (br ? p.woB : p.woA) + (size_t)(kvh * 512 + s * 32) * 512;
  const __amdgpu_buffer_rsrc_t rK = __builtin_amdgcn_make_buffer_rsrc((void*)kp, 0, 65536, RS_FLAGS);
  const __amdgpu_buffer_rsrc_t rV = __builtin_amdgcn_make_buffer_rsrc((void*)vp, 0, 65536, RS_FLAGS);
  const __amdgpu_buffer_rsrc_t rO = __builtin_amdgcn_make_buffer_rsrc((void*)wop, 0, 32768, RS_FLAGS);
  v4u kf[8], vf[8], wo[4];
  const int kvo = w * 8192 + (c16 >> 2) * 256 + g4 * 64 + (lane & 3) * 16;
  const int vvo = w * 8192 + lane * 16;
  const unsigned rot = (((unsigned)(b * 8 + w) * 2654435761u) >> 16);
#define KVOFF(i) ((int)(((i) + rot) & 7u) * 1024)
  if (PRIO) __builtin_amdgcn_s_setprio(2);
  const v2u ep = __builtin_amdgcn_raw_buffer_load_b64(rW, WS_EPOCH, 0, AUX_SC1);
  SB();
  __syncthreads();
  SB();
  if (KDELAY) __builtin_amdgcn_s_sleep(KDELAY);
  SB();
#pragma unroll
  for (int i = 0; i < 8; ++i) { if (i >= WND) waitvm<WND - 1>(); kf[i] = LD128(rK, kvo, KVOFF(i), AUX_NT); SB(); }
  const int wvo = (8 * (w & 3) + 4 * (w >> 2)) * 1024 + lane * 16;
#pragma unroll
  for (int j = 0; j < WOB; ++j) { waitvm<WND - 1>(); wo[j] = LD128(rO, wvo, j * 1024, AUX_NT); SB(); }
  STAMP(2);
  if (PRIO) __builtin_amdgcn_s_setprio(1);
#define QFLAG() (__builtin_amdgcn_readfirstlane(__hip_atomic_load(L_cnt + 1, __ATOMIC_RELAXED, __HIP_MEMORY_SCOPE_WORKGROUP)) == 4u)
  bool haveq = false;
#pragma unroll
  for (int i = 0; i < 8; ++i) {
    if (!haveq) { waitvm<WND - 1>(); haveq = QFLAG(); }
    SB();
    vf[i] = LD128(rV, vvo, KVOFF(i), AUX_NT);
    SB();
  }
  STAMP(3);
  if (PRIO) __builtin_amdgcn_s_setprio(2);
  const u64 e64 = (((u64)ep.y << 32) | ep.x) + 1ull;
  const unsigned tlo = (unsigned)e64 & 0xffu, thi = (unsigned)(e64 >> 8);
  while (!haveq) { haveq = QFLAG(); if (!haveq) __builtin_amdgcn_s_sleep(1); }
  asm volatile("" ::: "memory");
  STAMP(4);
  const v4u qf = *(const v4u*)(L_q + (c16 >> 2) * 64 + 4 * (4 * g4 + (c16 & 3)));
  float sc[8];
  {
    f32x4 d[8];
#pragma unroll
    for (int i = 0; i < 8; ++i) d[i] = mfma32(kf[i], qf, f32x4{0.f, 0.f, 0.f, 0.f});
#pragma unroll
    for (int i = 0; i < 8; ++i) sc[i] = (dpp<0x00>(d[i][0]) + dpp<0x55>(d[i][1])) + (dpp<0xAA>(d[i][2]) + dpp<0xFF>(d[i][3]));
  }
#pragma unroll
  for (int i = 0; i < 8; i += 2) {
    const unsigned r1 = cvtpk(sc[i], sc[i + 1]);
    const unsigned r2 = cvtpk(bflo(r1) * 0.08838834764831845f, bfhi(r1) * 0.08838834764831845f);
    sc[i] = bflo(r2); sc[i + 1] = bfhi(r2);
  }
  constexpr float LOG2E = 1.4426950408889634f;
  float m = fmaxf(fmaxf(fmaxf(sc[0], sc[1]), fmaxf(sc[2], sc[3])), fmaxf(fmaxf(sc[4], sc[5]), fmaxf(sc[6], sc[7])));
  m = max16(m); m = max32(m);
  float pe[8];
  float l = 0.f;
  {
    const float mb = m * LOG2E;
#pragma unroll
    for (int i = 0; i < 8; ++i) { pe[i] = __builtin_amdgcn_exp2f(sc[i] * LOG2E - mb); l += pe[i]; }
    l = sum16(l); l = sum32(l);
  }
  if (lane < 16 && !(lane & 3)) { L_m[(lane >> 2) * 8 + w] = m; L_l[(lane >> 2) * 8 + w] = l; }
  STAMP(5);
  __syncthreads();
  unsigned pk[4];
  {
    const f32x4 m0 = *(const f32x4*)(L_m + (c16 >> 2) * 8), m1 = *(const f32x4*)(L_m + (c16 >> 2) * 8 + 4);
    const float M = fmaxf(fmaxf(fmaxf(m0[0], m0[1]), fmaxf(m0[2], m0[3])), fmaxf(fmaxf(m1[0], m1[1]), fmaxf(m1[2], m1[3])));
    const float scale = __builtin_amdgcn_exp2f((m - M) * LOG2E);
#pragma unroll
    for (int i = 0; i < 4; ++i) pk[i] = cvtpk(pe[2 * i] * scale, pe[2 * i + 1] * scale);
  }
  {
    const int h = w >> 1;
    const float mw = L_m[h * 8 + (lane & 7)], lw = L_l[h * 8 + (lane & 7)];
    float M = fmaxf(mw, dpp<XOR1>(mw)); M = fmaxf(M, dpp<XOR2>(M)); M = fmaxf(M, dpp<HMIR>(M));
    float L = lw * __builtin_amdgcn_exp2f((mw - M) * LOG2E);
    L += dpp<XOR1>(L); L += dpp<XOR2>(L); L += dpp<HMIR>(L);
    const v4u gs = {g_lo(M, tlo), thi, g_lo(L, tlo), thi};
    ST128P(lane < 2, gs, rW, WS_O + ((grp * 16 + 2 * w + lane) * 16 + s) * MSG + 256, AUX_ST);
  }
  SB();
  STAMP(6);
  unsigned selE[2], selO[2];
  {
    const int jp = lane & 3;
#pragma unroll
    for (int r = 0; r < 2; ++r) {
      const bool on = (jp >> 1) == r;
      selE[r] = on ? ((jp & 1) ? 0x01000c0cu : 0x0c0c0100u) : 0x0c0c0c0cu;
      selO[r] = on ? ((jp & 1) ? 0x03020c0cu : 0x0c0c0302u) : 0x0c0c0c0cu;
    }
  }
  f32x4 acc0 = {0.f, 0.f, 0.f, 0.f}, acc1 = {0.f, 0.f, 0.f, 0.f};
#pragma unroll
  for (int i = 0; i < 8; ++i) {
    v2u a;
    a.x = __builtin_amdgcn_perm(pk[i >> 1], pk[i >> 1], (i & 1) ? selO[0] : selE[0]);
    a.y = __builtin_amdgcn_perm(pk[i >> 1], pk[i >> 1], (i & 1) ? selO[1] : selE[1]);
    acc0 = mfma16(a, v2u{vf[i].x, vf[i].y}, acc0);
    acc1 = mfma16(a, v2u{vf[i].z, vf[i].w}, acc1);
  }
  *(f32x4*)(L_o + w * 512 + g4 * 128 + 8 * c16) = acc0;
  *(f32x4*)(L_o + w * 512 + g4 * 128 + 8 * c16 + 4) = acc1;
  STAMP(7);
  __syncthreads();
  {
    float o0 = 0.f, o1 = 0.f;
#pragma unroll
    for (int ww = 0; ww < 4; ++ww) { const f32x2 t = *(const f32x2*)(L_o + (4 * (lane >> 5) + ww) * 512 + w * 64 + 2 * (lane & 31)); o0 += t.x; o1 += t.y; }
    o0 = sum32(o0); o1 = sum32(o1);
    const v4u g = {g_lo(o0, tlo), thi, g_lo(o1, tlo), thi};
    ST128P(lane < 32, g, rW, WS_O + ((grp * 16 + 2 * w + (lane >> 4)) * 16 + s) * MSG + (lane & 15) * 16, AUX_ST);
  }
  SB();
  STAMP(8);
#pragma unroll
  for (int j = WOB; j < 4; ++j) wo[j] = LD128(rO, wvo, j * 1024, AUX_NT);
  SB();
  {
    if (OACK) waitvm<4 - WOB>();
    if (ODELAY) __builtin_amdgcn_s_sleep(ODELAY);
    SB();
    STAMP(9);
    const int ooff = WS_O + ((grp * 16 + s) * 16 + (lane >> 2)) * MSG;
    const int doff = ooff + (w & 3) * 64 + (lane & 3) * 16;
    v4u od = LD128(rW, doff, 0, AUX_SC1);
    v4u sg = LD128(rW, ooff + 256, 0, AUX_SC1);
    SB();
    unsigned wl[8], wh[8];
#pragma unroll
    for (int k = 0; k < 2; ++k)
#pragma unroll
      for (int j = 0; j < 4; ++j) {
        wl[k * 4 + j] = __builtin_amdgcn_perm(wo[2 * k + 1][j], wo[2 * k][j], 0x05040100u);
        wh[k * 4 + j] = __builtin_amdgcn_perm(wo[2 * k + 1][j], wo[2 * k][j], 0x07060302u);
        asm volatile("" : "+v"(wl[k * 4 + j]), "+v"(wh[k * 4 + j]));
      }
    SB();
    for (unsigned spins = 0; spins < SPIN_CAP; ++spins) {
      const bool b0 = g_bad(od, tlo, thi) != 0u, b2 = g_bad(sg, tlo, thi) != 0u;
      if (!__any(b0 | b2)) break;
      __builtin_amdgcn_s_sleep(OSLEEP);
      if (b0) od = LD128(rW, doff, 0, AUX_SC1);
      if (b2) sg = LD128(rW, ooff + 256, 0, AUX_SC1);
    }
    STAMP(10);
    const float ms = g_val(sg.x), ls = g_val(sg.z);
    float M = fmaxf(ms, xor4(ms)); M = fmaxf(M, dpp<ROR8>(M)); M = max16(M); M = max32(M);
    const float wgt = __builtin_amdgcn_exp2f((ms - M) * LOG2E);
    float L = wgt * ls;
    float ov = tr32(wgt * g_val(od.x), wgt * g_val(od.z));
    L += xor4(L); ov += xor4(ov);
    L += dpp<ROR8>(L); ov += dpp<ROR8>(ov);
    L = sum16(L); ov = sum16(ov);
    L = sum32(L);
    ov *= __builtin_amdgcn_rcpf(L);
    const unsigned pd = cvtpk(ov, ov);
    const int l0 = 2 * (w >> 2);
    const unsigned p0 = __builtin_amdgcn_readlane(pd, l0), p1 = __builtin_amdgcn_readlane(pd, l0 + 32), p2 = __builtin_amdgcn_readlane(pd, l0 + 1), p3 = __builtin_amdgcn_readlane(pd, l0 + 33);
    const unsigned q01 = (p0 & 0xffffu) | (p1 & 0xffff0000u), q23 = (p2 & 0xffffu) | (p3 & 0xffff0000u);
    f32x4 f0, f1;
#pragma unroll
    for (int j = 0; j < 4; ++j) {
      const float lo = dot2(wl[4 + j], q23, dot2(wl[j], q01, 0.f)), hi = dot2(wh[4 + j], q23, dot2(wh[j], q01, 0.f));
      if (j < 2) { f0[2 * j] = lo; f0[2 * j + 1] = hi; } else { f1[2 * j - 4] = lo; f1[2 * j - 3] = hi; }
    }
    *(f32x4*)(L_o + w * 512 + 8 * lane) = f0;
    *(f32x4*)(L_o + w * 512 + 8 * lane + 4) = f1;
  }
  __syncthreads();
  {
    float f0 = 0.f, f1 = 0.f;
#pragma unroll
    for (int ww = 0; ww < 4; ++ww) { const f32x2 t = *(const f32x2*)(L_o + (4 * (lane >> 5) + ww) * 512 + w * 64 + 2 * (lane & 31)); f0 += t.x; f1 += t.y; }
    f0 = sum32(f0); f1 = sum32(f1);
    const v4u g = {g_lo(f0, tlo), thi, g_lo(f1, tlo), thi};
    ST128P(lane < 32, g, rW, WS_F + b * 4096 + (w * 64 + 2 * lane) * 8, AUX_ST);
  }
  STAMP(11);
  if (b < 32) {
    if (FACK) waitvm<0>();
    if (FDELAY) __builtin_amdgcn_s_sleep(FDELAY);
    SB();
    const int foff = WS_F + (32 * w + (lane >> 3)) * 4096 + b * 128 + (lane & 7) * 16;
    v4u fd[4];
#pragma unroll
    for (int i = 0; i < 4; ++i) fd[i] = LD128(rW, foff, i * 8 * 4096, AUX_SC1);
    for (unsigned spins = 0; spins < SPIN_CAP; ++spins) {
      bool bd[4];
#pragma unroll
      for (int i = 0; i < 4; ++i) bd[i] = g_bad(fd[i], tlo, thi) != 0u;
      if (!__any(bd[0] | bd[1] | bd[2] | bd[3])) break;
#pragma unroll
      for (int i = 0; i < 4; ++i) if (bd[i]) fd[i] = LD128(rW, foff, i * 8 * 4096, AUX_SC1);
      __builtin_amdgcn_s_sleep(1);
    }
    STAMP(12);
    float s0 = (g_val(fd[0].x) + g_val(fd[1].x)) + (g_val(fd[2].x) + g_val(fd[3].x));
    float s1 = (g_val(fd[0].z) + g_val(fd[1].z)) + (g_val(fd[2].z) + g_val(fd[3].z));
    s0 += dpp<ROR8>(s0); s1 += dpp<ROR8>(s1);
    const float t = sum32(tr16(s0, s1));
    if (lane < 8 || (lane >= 16 && lane < 24)) L_g[w * 16 + 2 * (lane & 7) + (lane >> 4)] = t;
    __syncthreads();
    if (w == 0) {
      const int c = lane & 15;
      const float a = (L_g[c] + L_g[16 + c]) + (L_g[32 + c] + L_g[48 + c]), bb = (L_g[64 + c] + L_g[80 + c]) + (L_g[96 + c] + L_g[112 + c]);
      const float gate = L_gate[0], omg = rbf(1.f - gate);
      const float r = rbf(gate * rbf(a)) + rbf(omg * rbf(bb));
      const float rn = dpp<XOR1>(r);
      if (lane < 16 && !(lane & 1)) ((unsigned*)p.out)[b * 8 + (lane >> 1)] = cvtpk(r, rn);
      if (b == 0 && lane == 0) *(u64*)(p.ws + WS_EPOCH) = e64;
    }
    STAMP(13);
  }
  if (tprobe) while (rt() - tprobe < 4000ull) __builtin_amdgcn_s_sleep(2);
}

extern "C" void kernel_launch(void* const* d_in, const int* in_sizes, int n_in, void* d_out, int out_size, void* d_ws, size_t ws_size, hipStream_t stream) {
  Params p;
  p.x = (const unsigned short*)d_in[0]; p.kA = (const unsigned short*)d_in[1]; p.vA = (const unsigned short*)d_in[2];
  p.wqA = (const unsigned short*)d_in[3]; p.woA = (const unsigned short*)d_in[4];
  p.kB = (const unsigned short*)d_in[6]; p.vB = (const unsigned short*)d_in[7];
  p.wqB = (const unsigned short*)d_in[8]; p.woB = (const unsigned short*)d_in[9]; p.gw = (const unsigned short*)d_in[10];
  p.out = (unsigned short*)d_out; p.ws = (unsigned char*)d_ws + ws_off(ws_size);
  (void)hipFuncSetAttribute((const void*)fused_decode, hipFuncAttributeMaxDynamicSharedMemorySize, LDS_BYTES);
  fused_decode<<<256, 768, LDS_BYTES, stream>>>(p);
}
```

```cpp
#include <hip/hip_runtime.h>
#include <stdint.h>

typedef unsigned v4u __attribute__((ext_vector_type(4)));
typedef unsigned v2u __attribute__((ext_vector_type(2)));
typedef float f32x4 __attribute__((ext_vector_type(4)));
typedef float f32x2 __attribute__((ext_vector_type(2)));
typedef short bf16x8 __attribute__((ext_vector_type(8)));
typedef short bf16x4 __attribute__((ext_vector_type(4)));
typedef __bf16 bf16x2 __attribute__((ext_vector_type(2)));
typedef unsigned long long u64;
#define SB() __builtin_amdgcn_sched_barrier(0)

constexpr int SK = 32768, WIN = 4096, DH = 128;
constexpr int AUX_NT = 2, RS_FLAGS = 0x00020000;
#define HLD 16
#define HST 16
constexpr int AUX_SC1 = HLD, AUX_ST = HST;
constexpr int WS_EPOCH = 0;
constexpr int WS_Q = 4096;
constexpr int MSG = 384;
constexpr int WS_O = WS_Q + 16 * 16 * 512;
constexpr int WS_F = WS_O + 16 * 16 * 16 * MSG;
constexpr int WS_END = WS_F + 256 * 512 * 8;
constexpr int WS_STAMP = 4 << 20;
constexpr unsigned SPIN_CAP = 1u << 14;
constexpr int LDS_BYTES = 96 * 1024;
#define WOB 4
#define ODELAY 4
#define FDELAY 4
#define OACK 0
#define OSLEEP 2
#define FACK 0
#define PRETOUCH 0
#define WND 3
#define KDELAY 0
#define QD1 8
#define QD2 20
#define PRIO 1
#define OWAIT 2

#define WSEND 1
static inline size_t ws_off(size_t ws_size) { return WSEND ? ((ws_size - (size_t)(WSEND) * (8u << 20)) & ~(size_t)0xfffff) : 0; }
struct Params {
  const unsigned short *x, *kA, *vA, *wqA, *woA, *kB, *vB, *wqB, *woB, *gw;
  unsigned short* out;
  unsigned char* ws;
};

__device__ __forceinline__ float dot2(unsigned a, unsigned b, float c) {
  return __builtin_amdgcn_fdot2_f32_bf16(__builtin_bit_cast(bf16x2, a), __builtin_bit_cast(bf16x2, b), c, false);
}
__device__ __forceinline__ unsigned cvtpk(float lo, float hi) {
  bf16x2 r = {(__bf16)lo, (__bf16)hi};
  return __builtin_bit_cast(unsigned, r);
}
__device__ __forceinline__ float bflo(unsigned u) { return __uint_as_float(u << 16); }
__device__ __forceinline__ float bfhi(unsigned u) { return __uint_as_float(u & 0xffff0000u); }
__device__ __forceinline__ float rbf(float x) { return bflo(cvtpk(x, x)); }
template <int CTRL> __device__ __forceinline__ float dpp(float x) {
  return __int_as_float(__builtin_amdgcn_mov_dpp(__float_as_int(x), CTRL, 0xf, 0xf, true));
}
template <int CTRL> __device__ __forceinline__ unsigned dppu(unsigned x) {
  return (unsigned)__builtin_amdgcn_mov_dpp((int)x, CTRL, 0xf, 0xf, true);
}
constexpr int XOR1 = 0xB1, XOR2 = 0x4E, HMIR = 0x141, MIR = 0x140, ROR8 = 0x128, SHL4 = 0x104, SHR4 = 0x114;
__device__ __forceinline__ float xor4(float x) {
  int a = __builtin_amdgcn_update_dpp(0, __float_as_int(x), SHL4, 0xf, 0x5, false);
  a = __builtin_amdgcn_update_dpp(a, __float_as_int(x), SHR4, 0xf, 0xa, false);
  return __int_as_float(a);
}
__device__ __forceinline__ float tr32(float a, float b) {
  auto r = __builtin_amdgcn_permlane32_swap(__float_as_uint(a), __float_as_uint(b), false, false);
  return __uint_as_float(r[0]) + __uint_as_float(r[1]);
}
__device__ __forceinline__ float tr16(float a, float b) {
  auto r = __builtin_amdgcn_permlane16_swap(__float_as_uint(a), __float_as_uint(b), false, false);
  return __uint_as_float(r[0]) + __uint_as_float(r[1]);
}
__device__ __forceinline__ float max32(float a) {
  auto r = __builtin_amdgcn_permlane32_swap(__float_as_uint(a), __float_as_uint(a), false, false);
  return fmaxf(__uint_as_float(r[0]), __uint_as_float(r[1]));
}
__device__ __forceinline__ float max16(float a) {
  auto r = __builtin_amdgcn_permlane16_swap(__float_as_uint(a), __float_as_uint(a), false, false);
  return fmaxf(__uint_as_float(r[0]), __uint_as_float(r[1]));
}
__device__ __forceinline__ float sum32(float a) { return tr32(a, a); }
__device__ __forceinline__ float sum16(float a) { return tr16(a, a); }
__device__ __forceinline__ f32x4 mfma32(v4u a, v4u b, f32x4 c) {
  return __builtin_amdgcn_mfma_f32_16x16x32_bf16(__builtin_bit_cast(bf16x8, a), __builtin_bit_cast(bf16x8, b), c, 0, 0, 0);
}
__device__ __forceinline__ f32x4 mfma16(v2u a, v2u b, f32x4 c) {
  return __builtin_amdgcn_mfma_f32_16x16x16bf16_1k(__builtin_bit_cast(bf16x4, a), __builtin_bit_cast(bf16x4, b), c, 0, 0, 0);
}
template <int N> __device__ __forceinline__ void waitvm() { SB(); asm volatile("s_waitcnt vmcnt(%0)" ::"i"(N) : "memory"); SB(); }
#define STAMP(i)

__device__ __forceinline__ unsigned g_lo(float v, unsigned tl) { return ((__float_as_uint(v) + 0x80u) & 0xffffff00u) | tl; }
__device__ __forceinline__ float g_val(unsigned lo) { return __uint_as_float(lo & 0xffffff00u); }
__device__ __forceinline__ unsigned g_bad(v4u g, unsigned tlo, unsigned thi) {
  return (g.y ^ thi) | (g.w ^ thi) | (((g.x ^ tlo) | (g.z ^ tlo)) & 0xffu);
}
#define LD128(rs, vo, so, aux) __builtin_amdgcn_raw_buffer_load_b128(rs, vo, so, aux)
#define ST128(v, rs, vo, aux) __builtin_amdgcn_raw_buffer_store_b128(v, rs, vo, 0, aux)
#define ST128P(pred, v, rs, vo, aux) __builtin_amdgcn_raw_buffer_store_b128(v, rs, (pred) ? (vo) : 0x7ffffff0, 0, aux)

__global__ void __launch_bounds__(768, 3) fused_decode(Params p) {
  extern __shared__ __attribute__((aligned(16))) unsigned char smem[];
  float* lds = (float*)smem;
  float* L_qred = lds;
  unsigned* L_cnt = (unsigned*)(lds + 256);
  float* L_gate = lds + 264;
  unsigned* L_q = (unsigned*)(lds + 512);
  float* L_m = lds + 768;
  float* L_l = lds + 800;
  float* L_g = lds + 1024;
  float* L_o = lds + 2048;
  STAMP(0);
  const int b = blockIdx.x, tid = threadIdx.x, lane = tid & 63;
  const int wv = __builtin_amdgcn_readfirstlane(tid >> 6);
  const int kvh = b & 7, br = b >> 7, s = (b >> 3) & 15, grp = br * 8 + kvh;
  const __amdgpu_buffer_rsrc_t rW = __builtin_amdgcn_make_buffer_rsrc((void*)p.ws, 0, WS_END, RS_FLAGS);
  const __amdgpu_buffer_rsrc_t rX = __builtin_amdgcn_make_buffer_rsrc((void*)p.x, 0, 1024, RS_FLAGS);
  const int c16 = lane & 15, g4 = lane >> 4;

  if (wv < 4) {
    const int a = wv;
    __builtin_amdgcn_s_setprio(3);
    const unsigned short* wqp = (br ? p.wqB : p.wqA) + (size_t)(s & 1) * 256 * 4096 + kvh * 512 + (s >> 1) * 64;
    const __amdgpu_buffer_rsrc_t rQ = __builtin_amdgcn_make_buffer_rsrc((void*)wqp, 0, 255 * 8192 + 128, RS_FLAGS);
    v4u wq[8];
    {
      const int vo = (64 * a + 8 * (lane >> 3)) * 8192 + (lane & 7) * 16;
#pragma unroll
      for (int i = 0; i < 8; ++i) wq[i] = LD128(rQ, vo, i * 8192, AUX_NT);
    }
    const v4u xf = LD128(rX, ((s & 1) * 256 + 64 * a + 8 * (lane >> 3)) * 2, 0, 0);
    const v2u ep = __builtin_amdgcn_raw_buffer_load_b64(rW, WS_EPOCH, 0, AUX_SC1);
    v4u xa, ga;
    if (a == 3) {
      const __amdgpu_buffer_rsrc_t rG = __builtin_amdgcn_make_buffer_rsrc((void*)p.gw, 0, 1024, RS_FLAGS);
      xa = LD128(rX, lane * 16, 0, 0); ga = LD128(rG, lane * 16, 0, 0);
    }
    v4u pt[3];
    if (PRETOUCH == 1) {
      const int ob = WS_O + (grp * 16 + s) * 16 * MSG;
      pt[0] = LD128(rW, ob + a * 1024 + lane * 16, 0, AUX_SC1);
      pt[1] = LD128(rW, (a < 2 ? ob + (4 + a) * 1024 : WS_Q + (grp * 16 + s) * 512 - 512 * (lane >> 5) + 0) + lane * 16, 0, AUX_SC1);
      pt[2] = LD128(rW, WS_F + b * 4096 + a * 1024 + lane * 16, 0, AUX_SC1);
    }
    if (PRETOUCH == 2 && a < 2) {
      const int off = a == 0 ? (lane < 48 ? WS_O + (grp * 16 + s) * 16 * MSG + lane * 128 : WS_Q + (grp * 16 + s) * 512 + (lane & 3) * 128)
                             : WS_F + b * 4096 + (lane & 31) * 128;
      if (a == 0 ? lane < 52 : lane < 32) asm volatile("global_atomic_add %0, %1, %2 sc1" ::"v"(off), "v"(0u), "s"(p.ws) : "memory");
    }
    SB();
    if (tid < 3) L_cnt[tid] = 0u;
    __syncthreads();
    SB();
    const u64 e64 = (((u64)ep.y << 32) | ep.x) + 1ull;
    const unsigned tlo = (unsigned)e64 & 0xffu, thi = (unsigned)(e64 >> 8);
    float acc[8];
#pragma unroll
    for (int j = 0; j < 8; ++j) acc[j] = 0.f;
#pragma unroll
    for (int k = 0; k < 4; ++k)
#pragma unroll
      for (int j = 0; j < 4; ++j) {
        const unsigned lo = __builtin_amdgcn_perm(wq[2 * k + 1][j], wq[2 * k][j], 0x05040100u);
        const unsigned hi = __builtin_amdgcn_perm(wq[2 * k + 1][j], wq[2 * k][j], 0x07060302u);
        acc[2 * j] = dot2(lo, xf[k], acc[2 * j]);
        acc[2 * j + 1] = dot2(hi, xf[k], acc[2 * j + 1]);
      }
    float a4[4], a2[2];
#pragma unroll
    for (int j = 0; j < 4; ++j) a4[j] = tr32(acc[j], acc[j + 4]);
#pragma unroll
    for (int j = 0; j < 2; ++j) a2[j] = tr16(a4[j], a4[j + 2]);
    const float t0 = a2[0] + dpp<ROR8>(a2[0]), t1 = a2[1] + dpp<ROR8>(a2[1]);
    const float a1 = (lane & 8) ? t1 : t0;
    const int col = 8 * (lane & 7) + ((lane >> 3) & 1) + 2 * ((lane >> 4) & 1) + 4 * (lane >> 5);
    L_qred[a * 64 + col] = a1;
    STAMP(1);
    unsigned old = 0;
    asm volatile("s_waitcnt lgkmcnt(0)" ::: "memory");
    if (lane == 0) old = __hip_atomic_fetch_add(L_cnt, 1u, __ATOMIC_RELAXED, __HIP_MEMORY_SCOPE_WORKGROUP);
    old = __builtin_amdgcn_readfirstlane(old);
    if (old == 3u) {
      asm volatile("" ::: "memory");
      float q0 = 0.f, q1 = 0.f;
#pragma unroll
      for (int ww = 0; ww < 2; ++ww) { const f32x2 t = *(const f32x2*)(L_qred + (2 * (lane >> 5) + ww) * 64 + 2 * (lane & 31)); q0 += t.x; q1 += t.y; }
      q0 = sum32(q0); q1 = sum32(q1);
      const v4u g = {g_lo(q0, tlo), thi, g_lo(q1, tlo), thi};
      if (lane < 32) ST128(g, rW, WS_Q + (grp * 16 + s) * 512 + lane * 16, AUX_ST);
      asm volatile("s_waitcnt vmcnt(0)" ::: "memory");
      if (lane == 0) __hip_atomic_store(L_cnt + 2, 1u, __ATOMIC_RELAXED, __HIP_MEMORY_SCOPE_WORKGROUP);
    }
    if (a == 3) {
      float gs = 0.f;
#pragma unroll
      for (int j = 0; j < 4; ++j) {
        const unsigned pr = cvtpk(bflo(xa[j]) * bflo(ga[j]), bfhi(xa[j]) * bfhi(ga[j]));
        gs += bflo(pr) + bfhi(pr);
      }
      gs += dpp<XOR1>(gs); gs += dpp<XOR2>(gs); gs += dpp<HMIR>(gs); gs += dpp<MIR>(gs);
      gs = sum16(gs); gs = sum32(gs);
      const float gate = rbf(__builtin_amdgcn_rcpf(1.f + __expf(-rbf(gs))));
      if (lane == 0) L_gate[0] = gate;
    }
    STAMP(2);
    const int qoff = WS_Q + (grp * 16 + 4 * a + 2 * (lane >> 5)) * 512 + (lane & 31) * 16;
    while (__builtin_amdgcn_readfirstlane(__hip_atomic_load(L_cnt + 2, __ATOMIC_RELAXED, __HIP_MEMORY_SCOPE_WORKGROUP)) == 0u) __builtin_amdgcn_s_sleep(1);
    __builtin_amdgcn_s_sleep(QD1);
    SB();
    v4u qa = LD128(rW, qoff, 0, AUX_SC1), qb = LD128(rW, qoff, 512, AUX_SC1);
    SB();
    __builtin_amdgcn_s_sleep(QD2);
    SB();
    v4u qc = LD128(rW, qoff, 0, AUX_SC1), qd = LD128(rW, qoff, 512, AUX_SC1);
    SB();
    STAMP(3);
    if (!__all((g_bad(qa, tlo, thi) | g_bad(qb, tlo, thi)) == 0u)) {
      if (g_bad(qa, tlo, thi)) qa = qc;
      if (g_bad(qb, tlo, thi)) qb = qd;
      for (unsigned spins = 0; spins < SPIN_CAP; ++spins) {
        const bool ba = g_bad(qa, tlo, thi) != 0u, bb = g_bad(qb, tlo, thi) != 0u;
        if (!__any(ba | bb)) break;
        if (ba) qa = LD128(rW, qoff, 0, AUX_SC1);
        if (bb) qb = LD128(rW, qoff, 512, AUX_SC1);
        __builtin_amdgcn_s_sleep(2);
      }
    }
    L_q[a * 64 + lane] = cvtpk(g_val(qa.x) + g_val(qb.x), g_val(qa.z) + g_val(qb.z));
    asm volatile("s_waitcnt lgkmcnt(0)" ::: "memory");
    if (lane == 0) __hip_atomic_fetch_add(L_cnt + 1, 1u, __ATOMIC_RELAXED, __HIP_MEMORY_SCOPE_WORKGROUP);
    if (PRETOUCH == 1) asm volatile("" ::"v"(pt[0]), "v"(pt[1]), "v"(pt[2]));
    if (PRETOUCH == 3 && a < 2) {
      const int off = a == 0 ? (lane < 48 ? WS_O + (grp * 16 + s) * 16 * MSG + lane * 128 : WS_EPOCH) : WS_F + b * 4096 + (lane & 31) * 128;
      const unsigned t = __builtin_amdgcn_raw_buffer_load_b32(rW, off, 0, AUX_SC1);
      asm volatile("" ::"v"(t));
    }
    STAMP(4);
    return;
  }
  const int w = wv - 4;
  const unsigned short* kp = (br ? p.kB : p.kA) + ((size_t)kvh * SK + (SK - WIN) + s * 256) * DH;
  const unsigned short* vp = (br ? p.vB : p.vA) + ((size_t)kvh * SK + (SK - WIN) + s * 256) * DH;
  const unsigned short* wop = (br ? p.woB : p.woA) + (size_t)(kvh * 512 + s * 32) * 512;
  const __amdgpu_buffer_rsrc_t rK = __builtin_amdgcn_make_buffer_rsrc((void*)kp, 0, 65536, RS_FLAGS);
  const __amdgpu_buffer_rsrc_t rV = __builtin_amdgcn_make_buffer_rsrc((void*)vp, 0, 65536, RS_FLAGS);
  const __amdgpu_buffer_rsrc_t rO = __builtin_amdgcn_make_buffer_rsrc((void*)wop, 0, 32768, RS_FLAGS);
  v4u kf[8], vf[8], wo[4];
  const int kvo = w * 8192 + (c16 >> 2) * 256 + g4 * 64 + (lane & 3) * 16;
  const int vvo = w * 8192 + lane * 16;
  const unsigned rot = (((unsigned)(b * 8 + w) * 2654435761u) >> 16);
#define KVOFF(i) ((int)(((i) + rot) & 7u) * 1024)
  if (PRIO) __builtin_amdgcn_s_setprio(2);
  const v2u ep = __builtin_amdgcn_raw_buffer_load_b64(rW, WS_EPOCH, 0, AUX_SC1);
  SB();
  __syncthreads();
  SB();
  if (KDELAY) __builtin_amdgcn_s_sleep(KDELAY);
  SB();
#pragma unroll
  for (int i = 0; i < 8; ++i) { if (i >= WND) waitvm<WND - 1>(); kf[i] = LD128(rK, kvo, KVOFF(i), AUX_NT); SB(); }
  const int wvo = (8 * (w & 3) + 4 * (w >> 2)) * 1024 + lane * 16;
#pragma unroll
  for (int j = 0; j < WOB; ++j) { waitvm<WND - 1>(); wo[j] = LD128(rO, wvo, j * 1024, AUX_NT); SB(); }
  STAMP(2);
  if (PRIO) __builtin_amdgcn_s_setprio(1);
#define QFLAG() (__builtin_amdgcn_readfirstlane(__hip_atomic_load(L_cnt + 1, __ATOMIC_RELAXED, __HIP_MEMORY_SCOPE_WORKGROUP)) == 4u)
  bool haveq = false;
#pragma unroll
  for (int i = 0; i < 8; ++i) {
    if (!haveq) { waitvm<WND - 1>(); haveq = QFLAG(); }
    SB();
    vf[i] = LD128(rV, vvo, KVOFF(i), AUX_NT);
    SB();
  }
  STAMP(3);
  if (PRIO) __builtin_amdgcn_s_setprio(2);
  const u64 e64 = (((u64)ep.y << 32) | ep.x) + 1ull;
  const unsigned tlo = (unsigned)e64 & 0xffu, thi = (unsigned)(e64 >> 8);
  while (!haveq) { haveq = QFLAG(); if (!haveq) __builtin_amdgcn_s_sleep(1); }
  asm volatile("" ::: "memory");
  STAMP(4);
  const v4u qf = *(const v4u*)(L_q + (c16 >> 2) * 64 + 4 * (4 * g4 + (c16 & 3)));
  float sc[8];
  {
    f32x4 d[8];
#pragma unroll
    for (int i = 0; i < 8; ++i) d[i] = mfma32(kf[i], qf, f32x4{0.f, 0.f, 0.f, 0.f});
#pragma unroll
    for (int i = 0; i < 8; ++i) sc[i] = (dpp<0x00>(d[i][0]) + dpp<0x55>(d[i][1])) + (dpp<0xAA>(d[i][2]) + dpp<0xFF>(d[i][3]));
  }
#pragma unroll
  for (int i = 0; i < 8; i += 2) {
    const unsigned r1 = cvtpk(sc[i], sc[i + 1]);
    const unsigned r2 = cvtpk(bflo(r1) * 0.08838834764831845f, bfhi(r1) * 0.08838834764831845f);
    sc[i] = bflo(r2); sc[i + 1] = bfhi(r2);
  }
  constexpr float LOG2E = 1.4426950408889634f;
  float m = fmaxf(fmaxf(fmaxf(sc[0], sc[1]), fmaxf(sc[2], sc[3])), fmaxf(fmaxf(sc[4], sc[5]), fmaxf(sc[6], sc[7])));
  m = max16(m); m = max32(m);
  float pe[8];
  float l = 0.f;
  {
    const float mb = m * LOG2E;
#pragma unroll
    for (int i = 0; i < 8; ++i) { pe[i] = __builtin_amdgcn_exp2f(sc[i] * LOG2E - mb); l += pe[i]; }
    l = sum16(l); l = sum32(l);
  }
  if (lane < 16 && !(lane & 3)) { L_m[(lane >> 2) * 8 + w] = m; L_l[(lane >> 2) * 8 + w] = l; }
  STAMP(5);
  __syncthreads();
  unsigned pk[4];
  {
    const f32x4 m0 = *(const f32x4*)(L_m + (c16 >> 2) * 8), m1 = *(const f32x4*)(L_m + (c16 >> 2) * 8 + 4);
    const float M = fmaxf(fmaxf(fmaxf(m0[0], m0[1]), fmaxf(m0[2], m0[3])), fmaxf(fmaxf(m1[0], m1[1]), fmaxf(m1[2], m1[3])));
    const float scale = __builtin_amdgcn_exp2f((m - M) * LOG2E);
#pragma unroll
    for (int i = 0; i < 4; ++i) pk[i] = cvtpk(pe[2 * i] * scale, pe[2 * i + 1] * scale);
  }
  {
    const int h = w >> 1;
    const float mw = L_m[h * 8 + (lane & 7)], lw = L_l[h * 8 + (lane & 7)];
    float M = fmaxf(mw, dpp<XOR1>(mw)); M = fmaxf(M, dpp<XOR2>(M)); M = fmaxf(M, dpp<HMIR>(M));
    float L = lw * __builtin_amdgcn_exp2f((mw - M) * LOG2E);
    L += dpp<XOR1>(L); L += dpp<XOR2>(L); L += dpp<HMIR>(L);
    const v4u gs = {g_lo(M, tlo), thi, g_lo(L, tlo), thi};
    ST128P(lane < 2, gs, rW, WS_O + ((grp * 16 + 2 * w + lane) * 16 + s) * MSG + 256, AUX_ST);
  }
  SB();
  STAMP(6);
  unsigned selE[2], selO[2];
  {
    const int jp = lane & 3;
#pragma unroll
    for (int r = 0; r < 2; ++r) {
      const bool on = (jp >> 1) == r;
      selE[r] = on ? ((jp & 1) ? 0x01000c0cu : 0x0c0c0100u) : 0x0c0c0c0cu;
      selO[r] = on ? ((jp & 1) ? 0x03020c0cu : 0x0c0c0302u) : 0x0c0c0c0cu;
    }
  }
  f32x4 acc0 = {0.f, 0.f, 0.f, 0.f}, acc1 = {0.f, 0.f, 0.f, 0.f};
#pragma unroll
  for (int i = 0; i < 8; ++i) {
    v2u a;
    a.x = __builtin_amdgcn_perm(pk[i >> 1], pk[i >> 1], (i & 1) ? selO[0] : selE[0]);
    a.y = __builtin_amdgcn_perm(pk[i >> 1], pk[i >> 1], (i & 1) ? selO[1] : selE[1]);
    acc0 = mfma16(a, v2u{vf[i].x, vf[i].y}, acc0);
    acc1 = mfma16(a, v2u{vf[i].z, vf[i].w}, acc1);
  }
  *(f32x4*)(L_o + w * 512 + g4 * 128 + 8 * c16) = acc0;
  *(f32x4*)(L_o + w * 512 + g4 * 128 + 8 * c16 + 4) = acc1;
  STAMP(7);
  __syncthreads();
  {
    float o0 = 0.f, o1 = 0.f;
#pragma unroll
    for (int ww = 0; ww < 4; ++ww) { const f32x2 t = *(const f32x2*)(L_o + (4 * (lane >> 5) + ww) * 512 + w * 64 + 2 * (lane & 31)); o0 += t.x; o1 += t.y; }
    o0 = sum32(o0); o1 = sum32(o1);
    const v4u g = {g_lo(o0, tlo), thi, g_lo(o1, tlo), thi};
    ST128P(lane < 32, g, rW, WS_O + ((grp * 16 + 2 * w + (lane >> 4)) * 16 + s) * MSG + (lane & 15) * 16, AUX_ST);
  }
  SB();
  STAMP(8);
#pragma unroll
  for (int j = WOB; j < 4; ++j) wo[j] = LD128(rO, wvo, j * 1024, AUX_NT);
  SB();
  {
    if (OACK) waitvm<4 - WOB>();
    if (ODELAY) __builtin_amdgcn_s_sleep(ODELAY);
    SB();
    STAMP(9);
    const int ooff = WS_O + ((grp * 16 + s) * 16 + (lane >> 2)) * MSG;
    const int doff = ooff + (w & 3) * 64 + (lane & 3) * 16;
    v4u od = LD128(rW, doff, 0, AUX_SC1);
    v4u sg = LD128(rW, ooff + 256, 0, AUX_SC1);
    SB();
    unsigned wl[8], wh[8];
#pragma unroll
    for (int k = 0; k < 2; ++k)
#pragma unroll
      for (int j = 0; j < 4; ++j) {
        wl[k * 4 + j] = __builtin_amdgcn_perm(wo[2 * k + 1][j], wo[2 * k][j], 0x05040100u);
        wh[k * 4 + j] = __builtin_amdgcn_perm(wo[2 * k + 1][j], wo[2 * k][j], 0x07060302u);
        asm volatile("" : "+v"(wl[k * 4 + j]), "+v"(wh[k * 4 + j]));
      }
    SB();
    for (unsigned spins = 0; spins < SPIN_CAP; ++spins) {
      const bool b0 = g_bad(od, tlo, thi) != 0u, b2 = g_bad(sg, tlo, thi) != 0u;
      if (!__any(b0 | b2)) break;
      __builtin_amdgcn_s_sleep(OSLEEP);
      if (b0) od = LD128(rW, doff, 0, AUX_SC1);
      if (b2) sg = LD128(rW, ooff + 256, 0, AUX_SC1);
    }
    STAMP(10);
    const float ms = g_val(sg.x), ls = g_val(sg.z);
    float M = fmaxf(ms, xor4(ms)); M = fmaxf(M, dpp<ROR8>(M)); M = max16(M); M = max32(M);
    const float wgt = __builtin_amdgcn_exp2f((ms - M) * LOG2E);
    float L = wgt * ls;
    float ov = tr32(wgt * g_val(od.x), wgt * g_val(od.z));
    L += xor4(L); ov += xor4(ov);
    L += dpp<ROR8>(L); ov += dpp<ROR8>(ov);
    L = sum16(L); ov = sum16(ov);
    L = sum32(L);
    ov *= __builtin_amdgcn_rcpf(L);
    const unsigned pd = cvtpk(ov, ov);
    const int l0 = 2 * (w >> 2);
    const unsigned p0 = __builtin_amdgcn_readlane(pd, l0), p1 = __builtin_amdgcn_readlane(pd, l0 + 32), p2 = __builtin_amdgcn_readlane(pd, l0 + 1), p3 = __builtin_amdgcn_readlane(pd, l0 + 33);
    const unsigned q01 = (p0 & 0xffffu) | (p1 & 0xffff0000u), q23 = (p2 & 0xffffu) | (p3 & 0xffff0000u);
    f32x4 f0, f1;
#pragma unroll
    for (int j = 0; j < 4; ++j) {
      const float lo = dot2(wl[4 + j], q23, dot2(wl[j], q01, 0.f)), hi = dot2(wh[4 + j], q23, dot2(wh[j], q01, 0.f));
      if (j < 2) { f0[2 * j] = lo; f0[2 * j + 1] = hi; } else { f1[2 * j - 4] = lo; f1[2 * j - 3] = hi; }
    }
    *(f32x4*)(L_o + w * 512 + 8 * lane) = f0;
    *(f32x4*)(L_o + w * 512 + 8 * lane + 4) = f1;
  }
  __syncthreads();
  {
    float f0 = 0.f, f1 = 0.f;
#pragma unroll
    for (int ww = 0; ww < 4; ++ww) { const f32x2 t = *(const f32x2*)(L_o + (4 * (lane >> 5) + ww) * 512 + w * 64 + 2 * (lane & 31)); f0 += t.x; f1 += t.y; }
    f0 = sum32(f0); f1 = sum32(f1);
    const v4u g = {g_lo(f0, tlo), thi, g_lo(f1, tlo), thi};
    ST128P(lane < 32, g, rW, WS_F + b * 4096 + (w * 64 + 2 * lane) * 8, AUX_ST);
  }
  STAMP(11);
  if (b < 32) {
    if (FACK) waitvm<0>();
    if (FDELAY) __builtin_amdgcn_s_sleep(FDELAY);
    SB();
    const int foff = WS_F + (32 * w + (lane >> 3)) * 4096 + b * 128 + (lane & 7) * 16;
    v4u fd[4];
#pragma unroll
    for (int i = 0; i < 4; ++i) fd[i] = LD128(rW, foff, i * 8 * 4096, AUX_SC1);
    for (unsigned spins = 0; spins < SPIN_CAP; ++spins) {
      bool bd[4];
#pragma unroll
      for (int i = 0; i < 4; ++i) bd[i] = g_bad(fd[i], tlo, thi) != 0u;
      if (!__any(bd[0] | bd[1] | bd[2] | bd[3])) break;
#pragma unroll
      for (int i = 0; i < 4; ++i) if (bd[i]) fd[i] = LD128(rW, foff, i * 8 * 4096, AUX_SC1);
      __builtin_amdgcn_s_sleep(1);
    }
    STAMP(12);
    float s0 = (g_val(fd[0].x) + g_val(fd[1].x)) + (g_val(fd[2].x) + g_val(fd[3].x));
    float s1 = (g_val(fd[0].z) + g_val(fd[1].z)) + (g_val(fd[2].z) + g_val(fd[3].z));
    s0 += dpp<ROR8>(s0); s1 += dpp<ROR8>(s1);
    const float t = sum32(tr16(s0, s1));
    if (lane < 8 || (lane >= 16 && lane < 24)) L_g[w * 16 + 2 * (lane & 7) + (lane >> 4)] = t;
    __syncthreads();
    if (w == 0) {
      const int c = lane & 15;
      const float a = (L_g[c] + L_g[16 + c]) + (L_g[32 + c] + L_g[48 + c]), bb = (L_g[64 + c] + L_g[80 + c]) + (L_g[96 + c] + L_g[112 + c]);
      const float gate = L_gate[0], omg = rbf(1.f - gate);
      const float r = rbf(gate * rbf(a)) + rbf(omg * rbf(bb));
      const float rn = dpp<XOR1>(r);
      if (lane < 16 && !(lane & 1)) ((unsigned*)p.out)[b * 8 + (lane >> 1)] = cvtpk(r, rn);
      if (b == 0 && lane == 0) *(u64*)(p.ws + WS_EPOCH) = e64;
    }
    STAMP(13);
  }
}

extern "C" void kernel_launch(void* const* d_in, const int* in_sizes, int n_in, void* d_out, int out_size, void* d_ws, size_t ws_size, hipStream_t stream) {
  Params p;
  p.x = (const unsigned short*)d_in[0]; p.kA = (const unsigned short*)d_in[1]; p.vA = (const unsigned short*)d_in[2];
  p.wqA = (const unsigned short*)d_in[3]; p.woA = (const unsigned short*)d_in[4];
  p.kB = (const unsigned short*)d_in[6]; p.vB = (const unsigned short*)d_in[7];
  p.wqB = (const unsigned short*)d_in[8]; p.woB = (const unsigned short*)d_in[9]; p.gw = (const unsigned short*)d_in[10];
  p.out = (unsigned short*)d_out; p.ws = (unsigned char*)d_ws + ws_off(ws_size);
  (void)hipFuncSetAttribute((const void*)fused_decode, hipFuncAttributeMaxDynamicSharedMemorySize, LDS_BYTES);
  fused_decode<<<256, 768, LDS_BYTES, stream>>>(p);
}
```

```cpp
#include <hip/hip_runtime.h>
#include <stdint.h>

typedef unsigned v4u __attribute__((ext_vector_type(4)));
typedef unsigned v2u __attribute__((ext_vector_type(2)));
typedef float f32x4 __attribute__((ext_vector_type(4)));
typedef float f32x2 __attribute__((ext_vector_type(2)));
typedef short bf16x8 __attribute__((ext_vector_type(8)));
typedef short bf16x4 __attribute__((ext_vector_type(4)));
typedef __bf16 bf16x2 __attribute__((ext_vector_type(2)));
typedef unsigned long long u64;
#define SB() __builtin_amdgcn_sched_barrier(0)
#define PIN(x) asm volatile("" : "+v"(x))

constexpr int SK = 32768, WIN = 4096, DH = 128;
constexpr int AUX_NT = 2, AUX_SC1 = 16, RS_FLAGS = 0x00020000;
constexpr int WS_EPOCH = 0;
constexpr int WS_T = 1024;
constexpr int WS_Q = 4096;
constexpr int MSG = 384;
constexpr int WS_O = WS_Q + 16 * 16 * 512;
constexpr int WS_F = WS_O + 16 * 16 * 16 * MSG;
constexpr int WS_END = WS_F + 256 * 512 * 8;
constexpr unsigned SPIN_CAP = 1u << 14;
constexpr int LDS_BYTES = 96 * 1024;
constexpr int OOB = 0x7ffffff0;

#define TK0 6
#define DK 16
#define TE 598
#define DVMIN 8
#define TW0 20
#define DW 13
#define RD 130
#define WF 6
#define WB 8
#define QD1 16
#define PSQ 12
#define ODELAY 8
#define PSH 3
#define PSS 6
#define FDELAY 10
#define PSF 6
#define SPQ 1
#define SPP 1
#define XQ 4
#define XS 4
#define FL 3
#define WSEND 1
static inline size_t ws_off(size_t ws_size) { return WSEND ? ((ws_size - (size_t)(WSEND) * (8u << 20)) & ~(size_t)0xfffff) : 0; }

struct Params {
  const unsigned short *x, *kA, *vA, *wqA, *woA, *kB, *vB, *wqB, *woB, *gw;
  unsigned short* out;
  unsigned char* ws;
};

__device__ __forceinline__ float dot2(unsigned a, unsigned b, float c) {
  return __builtin_amdgcn_fdot2_f32_bf16(__builtin_bit_cast(bf16x2, a), __builtin_bit_cast(bf16x2, b), c, false);
}
__device__ __forceinline__ unsigned cvtpk(float lo, float hi) {
  bf16x2 r = {(__bf16)lo, (__bf16)hi};
  return __builtin_bit_cast(unsigned, r);
}
__device__ __forceinline__ float bflo(unsigned u) { return __uint_as_float(u << 16); }
__device__ __forceinline__ float bfhi(unsigned u) { return __uint_as_float(u & 0xffff0000u); }
__device__ __forceinline__ float rbf(float x) { return bflo(cvtpk(x, x)); }
template <int CTRL> __device__ __forceinline__ float dpp(float x) {
  return __int_as_float(__builtin_amdgcn_mov_dpp(__float_as_int(x), CTRL, 0xf, 0xf, true));
}
constexpr int ROR1 = 0x121, ROR2 = 0x122, ROR4 = 0x124, ROR8 = 0x128, XOR1 = 0xB1, XOR2 = 0x4E, HMIR = 0x141, MIR = 0x140;
__device__ __forceinline__ float tr32(float a, float b) {
  auto r = __builtin_amdgcn_permlane32_swap(__float_as_uint(a), __float_as_uint(b), false, false);
  return __uint_as_float(r[0]) + __uint_as_float(r[1]);
}
__device__ __forceinline__ float tr16(float a, float b) {
  auto r = __builtin_amdgcn_permlane16_swap(__float_as_uint(a), __float_as_uint(b), false, false);
  return __uint_as_float(r[0]) + __uint_as_float(r[1]);
}
__device__ __forceinline__ float sum32(float a) { return tr32(a, a); }
__device__ __forceinline__ float sum16(float a) { return tr16(a, a); }
__device__ __forceinline__ float vmax(float a, float b) { float r; asm("v_max_f32 %0, %1, %2" : "=v"(r) : "v"(a), "v"(b)); return r; }
__device__ __forceinline__ float vmax3(float a, float b, float c) { float r; asm("v_max3_f32 %0, %1, %2, %3" : "=v"(r) : "v"(a), "v"(b), "v"(c)); return r; }
#define VMAX_DPP(name, ctrl) __device__ __forceinline__ float name(float a) { float r; asm("s_nop 1\n\tv_max_f32_dpp %0, %1, %1 " ctrl " row_mask:0xf bank_mask:0xf" : "=v"(r) : "v"(a)); return r; }
VMAX_DPP(vmax_ror1, "row_ror:1") VMAX_DPP(vmax_ror2, "row_ror:2") VMAX_DPP(vmax_ror4, "row_ror:4") VMAX_DPP(vmax_ror8, "row_ror:8")
VMAX_DPP(vmax_x1, "quad_perm:[1,0,3,2]") VMAX_DPP(vmax_x2, "quad_perm:[2,3,0,1]")
__device__ __forceinline__ float vmax16(float a) {
  auto r = __builtin_amdgcn_permlane16_swap(__float_as_uint(a), __float_as_uint(a), false, false);
  return vmax(__uint_as_float(r[0]), __uint_as_float(r[1]));
}
__device__ __forceinline__ float vmax32(float a) {
  auto r = __builtin_amdgcn_permlane32_swap(__float_as_uint(a), __float_as_uint(a), false, false);
  return vmax(__uint_as_float(r[0]), __uint_as_float(r[1]));
}
__device__ __forceinline__ f32x4 mfma32(v4u a, v4u b, f32x4 c) {
  return __builtin_amdgcn_mfma_f32_16x16x32_bf16(__builtin_bit_cast(bf16x8, a), __builtin_bit_cast(bf16x8, b), c, 0, 0, 0);
}
__device__ __forceinline__ f32x4 mfma16(v2u a, v2u b, f32x4 c) {
  return __builtin_amdgcn_mfma_f32_16x16x16bf16_1k(__builtin_bit_cast(bf16x4, a), __builtin_bit_cast(bf16x4, b), c, 0, 0, 0);
}
template <int N> __device__ __forceinline__ void waitvm() {
  SB(); __builtin_amdgcn_s_waitcnt((N & 0xf) | (0x7 << 4) | (0xf << 8) | ((N >> 4) << 14)); SB();
}
__device__ __forceinline__ u64 rt64() { u64 t; asm volatile("s_memrealtime %0\n s_waitcnt lgkmcnt(0)" : "=s"(t)::"memory"); return t; }
__device__ __forceinline__ unsigned rt32() { return (unsigned)rt64(); }
__device__ __forceinline__ void pace(unsigned target) { while ((int)(rt32() - target) < 0) __builtin_amdgcn_s_sleep(1); }

__device__ __forceinline__ unsigned g_lo(float v, unsigned tl) { return ((__float_as_uint(v) + 0x80u) & 0xffffff00u) | tl; }
#define LD128(rs, vo, so, aux) __builtin_amdgcn_raw_buffer_load_b128(rs, vo, so, aux)
#define ST128(v, rs, vo, aux) __builtin_amdgcn_raw_buffer_store_b128(v, rs, vo, 0, aux)
#define LDS_LD(ptr) __builtin_amdgcn_readfirstlane(__hip_atomic_load(ptr, __ATOMIC_RELAXED, __HIP_MEMORY_SCOPE_WORKGROUP))
#define LDS_ST(ptr, v) __hip_atomic_store(ptr, v, __ATOMIC_RELAXED, __HIP_MEMORY_SCOPE_WORKGROUP)
#define LDS_INC(ptr) __hip_atomic_fetch_add(ptr, 1u, __ATOMIC_RELAXED, __HIP_MEMORY_SCOPE_WORKGROUP)
#define LGKM0() asm volatile("s_waitcnt lgkmcnt(0)" ::: "memory")
#define SIG(i) do { u64 ex_; asm volatile("s_waitcnt lgkmcnt(0)\n\ts_mov_b64 %0, exec\n\ts_mov_b64 exec, 1\n\tds_add_u32 %1, %2 offset:%3\n\ts_mov_b64 exec, %0" \
    : "=&s"(ex_) : "v"(cnt_addr), "v"(cnt_one), "i"((i) * 4) : "memory"); } while (0)
#define SIG_RTN(dst, i) do { u64 ex_; unsigned r_; asm volatile("s_waitcnt lgkmcnt(0)\n\ts_mov_b64 %0, exec\n\ts_mov_b64 exec, 1\n\tds_add_rtn_u32 %1, %2, %3 offset:%4\n\ts_waitcnt lgkmcnt(0)\n\ts_mov_b64 exec, %0" \
    : "=&s"(ex_), "=&v"(r_) : "v"(cnt_addr), "v"(cnt_one), "i"((i) * 4) : "memory"); dst = __builtin_amdgcn_readfirstlane(r_); } while (0)

#define POLL_OK(A0, A1, A2, A3) \
  "v_cmpx_eq_u32_e32 vcc, %[thi], v" #A1 "\n\t" \
  "v_cmpx_eq_u32_e32 vcc, %[thi], v" #A3 "\n\t" \
  "v_perm_b32 %[tv], v" #A0 ", v" #A2 ", %[sel]\n\t" \
  "v_cmpx_eq_u32_e32 vcc, %[tl4], %[tv]\n\t"
#define POLL_VAL(D, A) "v_and_b32_e32 " D ", 0xffffff00, v" #A "\n\t"
#define POLL_NEXT "s_andn2_b64 %[mi], %[mi], exec\n\ts_cbranch_scc0 2f\n\ts_mov_b64 exec, %[mi]\n\t"
#define POLL_LD(A0, A3, OFF, SOFF, IMM) "buffer_load_dwordx4 v[" #A0 ":" #A3 "], " OFF ", %[rs], " SOFF " offen" IMM " sc1\n\t"
#define POLL_END "s_sub_u32 %[cnt], %[cnt], 1\n\ts_cbranch_scc0 1b\n2:\n\ts_mov_b64 exec, %[ex]\n\ts_nop 1"
#define POLL1_ROUND(W, A0, A1, A2, A3) \
  "s_waitcnt vmcnt(" W ")\n\t" POLL_OK(A0, A1, A2, A3) POLL_VAL("%[x0]", A0) POLL_VAL("%[x1]", A2) POLL_NEXT POLL_LD(A0, A3, "%[off]", "0", "")

enum { C_QP = 0, C_QD = 1, C_KI = 3, C_P = 4, C_QS = 5, C_L = 6, C_TEOK = 8, C_TE = 9, C_OM = 10, C_VI = 12, C_VS = 13, C_N = 16 };

__global__ void __launch_bounds__(1024, 4) fused_decode(Params p) {
  extern __shared__ __attribute__((aligned(16))) unsigned char smem[];
  float* lds = (float*)smem;
  float* L_qred = lds;
  unsigned* L_cnt = (unsigned*)(lds + 256);
  float* L_m = lds + 272;
  float* L_l = lds + 288;
  float* L_om = lds + 304;
  float* L_gate = lds + 320;
  unsigned* L_q = (unsigned*)(lds + 512);
  float* L_g = lds + 768;
  unsigned* L_p = (unsigned*)(lds + 1024);
  float* L_o = lds + 3072;
  float* L_f = lds + 8192;
  unsigned cpt;
  {
    u64 pc; int len;
    asm volatile("s_getpc_b64 %0\n.Lcode_here:\n\ts_mov_b32 %1, .Lcode_e0-.Lcode_here\n\ts_max_i32 %1, %1, .Lcode_e1-.Lcode_here\n\ts_max_i32 %1, %1, .Lcode_e2-.Lcode_here\n\ts_max_i32 %1, %1, .Lcode_e3-.Lcode_here"
                 : "=s"(pc), "=s"(len) :: "scc");
    cpt = __builtin_amdgcn_raw_buffer_load_b32(__builtin_amdgcn_make_buffer_rsrc((void*)pc, 0, len, RS_FLAGS), (int)threadIdx.x * 128 - 13 * 8192, 0, 0);
    SB();
  }
  asm volatile("" :: "s"(p.ws));
  SB();
  const u64 tz64 = rt64();
  const unsigned tz = (unsigned)tz64;
  const int b = blockIdx.x, tid = threadIdx.x, lane = tid & 63;
  const int wv = __builtin_amdgcn_readfirstlane(tid >> 6);
  const int kvh = b & 7, br = b >> 7, s = (b >> 3) & 15, grp = br * 8 + kvh;
  const __amdgpu_buffer_rsrc_t rW = __builtin_amdgcn_make_buffer_rsrc((void*)p.ws, 0, WS_END, RS_FLAGS);
  const v4u rsW = {(unsigned)(uintptr_t)p.ws, (unsigned)((uintptr_t)p.ws >> 32) & 0xffffu, (unsigned)WS_END, (unsigned)RS_FLAGS};
  const int mbox = WS_O + ((br * 8 + ((kvh + XS) & 7)) * 16 + s) * 16 * MSG;
  const int c16 = lane & 15, g4 = lane >> 4;
  unsigned cnt_addr = (unsigned)(uintptr_t)(__attribute__((address_space(3))) unsigned*)L_cnt, cnt_one = 1u;
  PIN(cnt_addr); PIN(cnt_one);
  constexpr float LOG2E = 1.4426950408889634f;
#define ROT(v) (((unsigned)(b * 8 + (v)) * 2654435761u) >> 16)

  if (wv >= 8) {
  const int w = wv - 8;
  const unsigned short* vp = (br ? p.vB : p.vA) + ((size_t)kvh * SK + (SK - WIN) + s * 256) * DH;
  const __amdgpu_buffer_rsrc_t rV = __builtin_amdgcn_make_buffer_rsrc((void*)vp, 0, 65536, RS_FLAGS);
  v4u vf[8];
  const int vvo = w * 8192 + lane * 16;
  const unsigned rot = ROT(w);
#define VOFF(i) ((int)(((i) + rot) & 7u) * 1024)
  const v2u ep = __builtin_amdgcn_raw_buffer_load_b64(rW, WS_EPOCH, 0, AUX_SC1);
  if (w == 0 && b < 8) { const v4u tv = {(unsigned)tz64, (unsigned)(tz64 >> 32), (unsigned)tz64, (unsigned)(tz64 >> 32)}; ST128(tv, rW, lane < 8 ? WS_T + b * 128 + lane * 16 : OOB, AUX_SC1); }
  SB();
  __syncthreads();
  SB();
  asm volatile("" :: "v"(cpt));
  if (w == 0) {
    pace(tz + RD);
    const v2u ts = __builtin_amdgcn_raw_buffer_load_b64(rW, lane < 8 ? WS_T + lane * 128 : OOB, 0, AUX_SC1);
    const u64 now = rt64();
    unsigned oldest = (unsigned)(now - tz64), sum = 0;
#pragma unroll
    for (int i = 0; i < 8; ++i) {
      const u64 d = now - (((u64)(unsigned)__builtin_amdgcn_readlane(ts.y, i) << 32) | (unsigned)__builtin_amdgcn_readlane(ts.x, i));
      const unsigned dd = d < 600ull ? (unsigned)d : 0u;
      oldest = dd > oldest ? dd : oldest; sum += dd;
    }
    const unsigned te = (unsigned)now - oldest + TE + (oldest - (sum >> 3));
    if (lane == 0) LDS_ST(L_cnt + C_TE, te);
    SIG(C_TEOK);
    asm volatile(".Lcode_e2:");
  }
  const u64 e64 = (((u64)ep.y << 32) | ep.x) + 1ull;
  unsigned tlo = (unsigned)e64 & 0xffu, thi = (unsigned)(e64 >> 8);
  int oaddr = lane < 32 ? WS_O + ((grp * 16 + 2 * w + (lane >> 4)) * 16 + s) * MSG + (lane & 15) * 16 : OOB;
  int hoff = mbox + c16 * MSG + 256;
  int a_p = ((w * 4 + g4) * 4 + (c16 >> 2)) * 4, a_m = g4 * 4, a_ow = w * 512 + g4 * 128 + 8 * c16, a_or = (4 * (lane >> 5)) * 512 + w * 64 + 2 * (lane & 31);
  unsigned selE[2], selO[2];
  {
    const int jp = lane & 3;
#pragma unroll
    for (int r = 0; r < 2; ++r) {
      const bool on = (jp >> 1) == r;
      selE[r] = on ? ((jp & 1) ? 0x01000c0cu : 0x0c0c0100u) : 0x0c0c0c0cu;
      selO[r] = on ? ((jp & 1) ? 0x03020c0cu : 0x0c0c0302u) : 0x0c0c0c0cu;
    }
  }
  PIN(tlo); PIN(thi); PIN(oaddr); PIN(hoff); PIN(a_p); PIN(a_m); PIN(a_ow); PIN(a_or); PIN(selE[0]); PIN(selE[1]); PIN(selO[0]); PIN(selO[1]);
  SB();
  while (LDS_LD(L_cnt + C_KI) != 4u) __builtin_amdgcn_s_sleep(2);
  while (LDS_LD(L_cnt + C_TEOK) == 0u) __builtin_amdgcn_s_sleep(2);
  {
    const unsigned te = LDS_LD(L_cnt + C_TE), tv0 = rt32();
    const int span = (int)(te - tv0);
    const unsigned dv = span > 8 * DVMIN ? (unsigned)span / 8u : (unsigned)DVMIN;
#pragma unroll
    for (int i = 0; i < 8; ++i) { if (i >= WB) waitvm<(WB > 8 ? 8 : WB) - 1>(); pace(tv0 + i * dv + ((w * dv) >> 3)); vf[i] = LD128(rV, vvo, VOFF(i), AUX_NT); SB(); }
  }
  SIG(C_VI);
  while (LDS_LD(L_cnt + C_P) != 4u) { if (SPP) __builtin_amdgcn_s_sleep(SPP); asm volatile("" ::: "memory"); }
  asm volatile("" ::: "memory");
  const v4u pk = *(const v4u*)(L_p + a_p);
  const f32x4 mm = *(const f32x4*)(L_m + a_m);
  f32x4 acc0 = {0.f, 0.f, 0.f, 0.f}, acc1 = acc0, acc2 = acc0, acc3 = acc0;
#pragma unroll
  for (int i = 0; i < 8; ++i) {
    v2u a;
    a.x = __builtin_amdgcn_perm(pk[i >> 1], pk[i >> 1], (i & 1) ? selO[0] : selE[0]);
    a.y = __builtin_amdgcn_perm(pk[i >> 1], pk[i >> 1], (i & 1) ? selO[1] : selE[1]);
    if (i & 1) { acc2 = mfma16(a, v2u{vf[i].x, vf[i].y}, acc2); acc3 = mfma16(a, v2u{vf[i].z, vf[i].w}, acc3); }
    else       { acc0 = mfma16(a, v2u{vf[i].x, vf[i].y}, acc0); acc1 = mfma16(a, v2u{vf[i].z, vf[i].w}, acc1); }
  }
  {
    const float M = vmax(vmax3(mm[0], mm[1], mm[2]), mm[3]);
    const int k = w >> 1;
    const float oscale = __builtin_amdgcn_exp2f(((k == 0 ? mm[0] : k == 1 ? mm[1] : k == 2 ? mm[2] : mm[3]) - M) * LOG2E);
    *(f32x4*)(L_o + a_ow) = (acc0 + acc2) * oscale;
    *(f32x4*)(L_o + a_ow + 4) = (acc1 + acc3) * oscale;
  }
  v4u hs0 = {0, 0, 0, 0};
  if (w == 0) {
    u64 ex;
    asm volatile("s_mov_b64 %[ex], exec\n\ts_mov_b64 exec, 0xffff\n\t" POLL_LD(104, 107, "%[off]", "0", "") "s_mov_b64 exec, %[ex]"
                 : "=&{v[104:107]}"(hs0), [ex] "=&s"(ex) : [off] "v"(hoff), [rs] "s"(rsW) : "memory");
  }
  SIG(C_VS);
  while (LDS_LD(L_cnt + C_VS) != 8u) asm volatile("" ::: "memory");
  asm volatile("" ::: "memory");
  {
    const f32x2 t0 = *(const f32x2*)(L_o + a_or), t1 = *(const f32x2*)(L_o + a_or + 512), t2 = *(const f32x2*)(L_o + a_or + 1024), t3 = *(const f32x2*)(L_o + a_or + 1536);
    const f32x2 ts = (t0 + t1) + (t2 + t3);
    const float o0 = sum32(ts.x), o1 = sum32(ts.y);
    const v4u g = {g_lo(o0, tlo), thi, g_lo(o1, tlo), thi};
    ST128(g, rW, oaddr, AUX_SC1);
  }
  SB();
  if (w == 0) {
    const unsigned sthi = __builtin_amdgcn_readfirstlane(thi), stl4 = __builtin_amdgcn_readfirstlane(tlo) * 0x01010101u;
    v4u t0 = hs0, t1, t2; float ms = 0.f, ls = 0.f;
    {
      u64 ex, mi; unsigned sel, cnt, tv;
      asm volatile(
        "s_mov_b64 %[ex], exec\n\ts_mov_b64 %[mi], 0xffff\n\ts_mov_b64 exec, 0xffff\n\ts_mov_b32 %[sel], 0x04000400\n\ts_mov_b32 %[cnt], %[cap]\n\t"
        POLL1_ROUND("1", 104, 105, 106, 107)
        "s_sleep %[ps]\n\t" POLL_LD(108, 111, "%[off]", "0", "") "s_sleep %[ps]\n\t" POLL_LD(112, 115, "%[off]", "0", "")
        "1:\n\t"
        POLL1_ROUND("2", 104, 105, 106, 107)
        POLL1_ROUND("2", 108, 109, 110, 111)
        POLL1_ROUND("2", 112, 113, 114, 115)
        POLL_END
        : "+{v[104:107]}"(t0), "=&{v[108:111]}"(t1), "=&{v[112:115]}"(t2), [x0] "+&v"(ms), [x1] "+&v"(ls), [tv] "=&v"(tv),
          [ex] "=&s"(ex), [mi] "=&s"(mi), [sel] "=&s"(sel), [cnt] "=&s"(cnt)
        : [off] "v"(hoff), [rs] "s"(rsW), [thi] "s"(sthi), [tl4] "s"(stl4), [cap] "i"(SPIN_CAP), [ps] "i"(PSS)
        : "vcc", "memory");
    }
    float M = vmax_ror1(ms); M = vmax_ror2(M); M = vmax_ror4(M); M = vmax_ror8(M);
    const float wg = __builtin_amdgcn_exp2f((ms - M) * LOG2E);
    float L = wg * ls;
    L += dpp<ROR1>(L); L += dpp<ROR2>(L); L += dpp<ROR4>(L); L += dpp<ROR8>(L);
    if (lane < 16) L_om[lane] = wg * __builtin_amdgcn_rcpf(L);
    SIG(C_OM);
    asm volatile(".Lcode_e3:");
    asm volatile("" :: "{v[104:107]}"(t0), "{v[108:111]}"(t1), "{v[112:115]}"(t2) : "memory");
  }
  return;
  }
  if (wv >= 4) {
    const int f = wv - 4;
    __builtin_amdgcn_s_setprio(2);
    const unsigned short* kp = (br ? p.kB : p.kA) + ((size_t)kvh * SK + (SK - WIN) + s * 256) * DH;
    const __amdgpu_buffer_rsrc_t rK = __builtin_amdgcn_make_buffer_rsrc((void*)kp, 0, 65536, RS_FLAGS);
    v4u kf[16];
    const v2u ep = __builtin_amdgcn_raw_buffer_load_b64(rW, WS_EPOCH, 0, AUX_SC1);
    SB();
    __syncthreads();
    SB();
    asm volatile("" :: "v"(cpt));
    const int kvo = f * 16384 + (c16 >> 2) * 256 + g4 * 64 + (lane & 3) * 16;
    const unsigned rot0 = ROT(2 * f), rot1 = ROT(2 * f + 1);
#define KOFF(i) ((int)(((i) < 8 ? (((i) + rot0) & 7u) : (8u + (((i) + rot1) & 7u))) * 1024))
#pragma unroll
    for (int n = 0; n < 16; ++n) {
      const int i = 8 * (n & 1) + (n >> 1);
      if (n >= WF) waitvm<WF - 1>();
      pace(tz + TK0 + n * DK + f * (DK / 4));
      kf[i] = LD128(rK, kvo, KOFF(i), AUX_NT); SB();
    }
    SIG(C_KI);
    const u64 e64 = (((u64)(unsigned)__builtin_amdgcn_readfirstlane(ep.y) << 32) | (unsigned)__builtin_amdgcn_readfirstlane(ep.x)) + 1ull;
    const unsigned tlo = (unsigned)e64 & 0xffu, thi = (unsigned)(e64 >> 8), tl4 = tlo * 0x01010101u;
    int a_q = (c16 >> 2) * 64 + 4 * (4 * g4 + (c16 & 3));
    int a_p = ((2 * f * 4 + g4) * 4 + (c16 >> 2)) * 8 + (lane & 3);
    int doff = mbox + (lane >> 2) * MSG + f * 64 + (lane & 3) * 16;
    int fpo = FL == 0 ? WS_F + b * 4096 + f * 1024 + lane * 16 : WS_F + (16 * f + (lane >> 2)) * 16384 + b * 64 + (lane & 3) * 16;
    int a_fw = f * 544 + 4 * lane;
    int a_fr = ((lane >> 1) & 1) * 272 + (16 * f + (lane >> 2)) * 4 + 2 * (lane & 1);
    int foff = FL == 0 ? WS_F + (64 * f + (lane >> 2)) * 4096 + b * 64 + (lane & 3) * 16 : WS_F + b * 16384 + (64 * f + (lane >> 2)) * 64 + (lane & 3) * 16;
    int wvo = f * 8192 + lane * 16;
    PIN(a_q); PIN(a_p); PIN(doff); PIN(fpo); PIN(a_fw); PIN(a_fr); PIN(foff); PIN(wvo);
    while (LDS_LD(L_cnt + C_QD) != 4u) { if (SPQ) __builtin_amdgcn_s_sleep(SPQ); asm volatile("" ::: "memory"); }
    asm volatile("" ::: "memory");
    const v4u qf = *(const v4u*)(L_q + a_q);
    float sc[16];
#pragma unroll
    for (int hf = 0; hf < 2; ++hf) {
      f32x4 d[8];
#pragma unroll
      for (int i = 0; i < 8; ++i) d[i] = mfma32(kf[8 * hf + i], qf, f32x4{0.f, 0.f, 0.f, 0.f});
#pragma unroll
      for (int i = 0; i < 8; ++i) sc[8 * hf + i] = dpp<0xFF>(d[i][3]) + (dpp<0xAA>(d[i][2]) + (dpp<0x55>(d[i][1]) + dpp<0x00>(d[i][0])));
    }
    unsigned parr;
    {
      const bool b0 = lane & 1, b1 = lane & 2;
      float x[4];
#pragma unroll
      for (int k = 0; k < 4; ++k) {
        float s0 = sc[4 * k], s1 = sc[4 * k + 1], s2 = sc[4 * k + 2], s3 = sc[4 * k + 3];
        asm("" : "+v"(s0), "+v"(s1), "+v"(s2), "+v"(s3));
        const float u = b0 ? s1 : s0, v = b0 ? s3 : s2;
        x[k] = b1 ? v : u;
      }
#pragma unroll
      for (int k = 0; k < 4; k += 2) {
        const unsigned r1 = cvtpk(x[k], x[k + 1]);
        const unsigned r2 = cvtpk(bflo(r1) * 0.08838834764831845f, bfhi(r1) * 0.08838834764831845f);
        x[k] = bflo(r2); x[k + 1] = bfhi(r2);
      }
      float m = vmax(vmax3(x[0], x[1], x[2]), x[3]);
      m = vmax_x1(m); m = vmax_x2(m); m = vmax16(m); m = vmax32(m);
      const float mb = m * LOG2E;
      float pr[4];
#pragma unroll
      for (int k = 0; k < 4; ++k) pr[k] = __builtin_amdgcn_exp2f(x[k] * LOG2E - mb);
      const unsigned p01 = cvtpk(pr[0], pr[1]), p23 = cvtpk(pr[2], pr[3]);
      unsigned short* dst = (unsigned short*)L_p + a_p;
      dst[0] = (unsigned short)p01; dst[4] = (unsigned short)(p01 >> 16);
      dst[128] = (unsigned short)p23; dst[132] = (unsigned short)(p23 >> 16);
      if (lane < 16 && !(lane & 3)) L_m[(lane >> 2) * 4 + f] = m;
      SIG(C_P);
      SB();
      float l = (pr[0] + pr[1]) + (pr[2] + pr[3]);
      l += dpp<XOR1>(l); l += dpp<XOR2>(l);
      l = sum16(l); l = sum32(l);
      if (lane < 16 && !(lane & 3)) L_l[(lane >> 2) * 4 + f] = l;
      SIG_RTN(parr, C_L);
    }
    if (parr == 3u) {
      asm volatile("" ::: "memory");
#pragma unroll
      for (int i = 0; i < 2; ++i) {
        const int h = 2 * i + (lane >> 5);
        const f32x4 mm = *(const f32x4*)(L_m + h * 4), ll = *(const f32x4*)(L_l + h * 4);
        const float M = vmax(vmax3(mm[0], mm[1], mm[2]), mm[3]);
        float L = 0.f;
#pragma unroll
        for (int k = 0; k < 4; ++k) L += ll[k] * __builtin_amdgcn_exp2f((mm[k] - M) * LOG2E);
        const v4u gs = {g_lo(M, tlo), thi, g_lo(L, tlo), thi};
        ST128(gs, rW, WS_O + ((grp * 16 + 8 * i + (lane >> 3)) * 16 + s) * MSG + 256 + (lane & 7) * 16, AUX_SC1);
      }
    }
    v4u R0, R1, R2, R3, R4, R5, R6, R7;
    const unsigned xr = ROT(16 + f) & 7u;
    {
      const unsigned short* wop = (br ? p.woB : p.woA) + (size_t)(((kvh + XS) & 7) * 512 + s * 32) * 512;
      const v4u rsO = {(unsigned)(uintptr_t)wop, (unsigned)((uintptr_t)wop >> 32) & 0xffffu, 32768u, (unsigned)RS_FLAGS};
      while (LDS_LD(L_cnt + C_VI) != 8u) __builtin_amdgcn_s_sleep(1);
      unsigned tw = rt32();
      { const unsigned te = LDS_LD(L_cnt + C_TE) + TW0; if ((int)(te - tw) > 0) tw = te; }
      tw += f * (DW / 4);
#define WO_LD(J, RJ, A0, A3) do { pace(tw + J * DW); \
        asm volatile("buffer_load_dwordx4 v[" #A0 ":" #A3 "], %[vo], %[rs], %[so] offen nt" : "={v[" #A0 ":" #A3 "]}"(RJ) : [vo] "v"(wvo), [rs] "s"(rsO), [so] "s"((J ^ xr) * 1024u) : "memory"); SB(); } while (0)
      WO_LD(0u, R0, 64, 67); WO_LD(1u, R1, 68, 71); WO_LD(2u, R2, 72, 75); WO_LD(3u, R3, 76, 79);
      WO_LD(4u, R4, 80, 83); WO_LD(5u, R5, 84, 87); WO_LD(6u, R6, 88, 91); WO_LD(7u, R7, 92, 95);
    }
    SB();
    while (LDS_LD(L_cnt + C_VS) != 8u) __builtin_amdgcn_s_sleep(1);
    SB();
    __builtin_amdgcn_s_sleep(ODELAY);
    SB();
    float xa, xb, om;
    v4u t0, t1, t2, W[8];
    {
      u64 ex, mi; unsigned sel, cnt, tv;
      asm volatile(
        POLL_LD(104, 107, "%[off]", "0", "") "s_sleep %[ps]\n\t" POLL_LD(108, 111, "%[off]", "0", "") "s_sleep %[ps]\n\t" POLL_LD(112, 115, "%[off]", "0", "")
        : "=&{v[104:107]}"(t0), "=&{v[108:111]}"(t1), "=&{v[112:115]}"(t2)
        : [off] "v"(doff), [rs] "s"(rsW), [ps] "i"(PSH) : "memory");
      SB();
      asm volatile("s_waitcnt vmcnt(3)" : "+{v[64:67]}"(R0), "+{v[68:71]}"(R1), "+{v[72:75]}"(R2), "+{v[76:79]}"(R3), "+{v[80:83]}"(R4), "+{v[84:87]}"(R5), "+{v[88:91]}"(R6), "+{v[92:95]}"(R7) :: "memory");
      {
        const unsigned sl = (xr & 1) ? 0x01000504u : 0x05040100u, sh = (xr & 1) ? 0x03020706u : 0x07060302u;
#pragma unroll
        for (int j = 0; j < 4; ++j) {
          W[0][j] = __builtin_amdgcn_perm(R1[j], R0[j], sl); W[1][j] = __builtin_amdgcn_perm(R1[j], R0[j], sh);
          W[2][j] = __builtin_amdgcn_perm(R3[j], R2[j], sl); W[3][j] = __builtin_amdgcn_perm(R3[j], R2[j], sh);
          W[4][j] = __builtin_amdgcn_perm(R5[j], R4[j], sl); W[5][j] = __builtin_amdgcn_perm(R5[j], R4[j], sh);
          W[6][j] = __builtin_amdgcn_perm(R7[j], R6[j], sl); W[7][j] = __builtin_amdgcn_perm(R7[j], R6[j], sh);
        }
#pragma unroll
        for (int i = 0; i < 8; ++i) PIN(W[i]);
      }
      SB();
      while (LDS_LD(L_cnt + C_OM) == 0u) __builtin_amdgcn_s_sleep(1);
      asm volatile("" ::: "memory");
      om = L_om[lane >> 2];
      PIN(om);
      SB();
      asm volatile(
        "s_mov_b64 %[ex], exec\n\ts_mov_b64 %[mi], exec\n\ts_mov_b32 %[sel], 0x04000400\n\ts_mov_b32 %[cnt], %[cap]\n"
        "1:\n\t"
        POLL1_ROUND("2", 104, 105, 106, 107)
        POLL1_ROUND("2", 108, 109, 110, 111)
        POLL1_ROUND("2", 112, 113, 114, 115)
        POLL_END
        : "+{v[104:107]}"(t0), "+{v[108:111]}"(t1), "+{v[112:115]}"(t2),
          [x0] "=&v"(xa), [x1] "=&v"(xb), [tv] "=&v"(tv),
          [ex] "=&s"(ex), [mi] "=&s"(mi), [sel] "=&s"(sel), [cnt] "=&s"(cnt)
        : [off] "v"(doff), [rs] "s"(rsW), [thi] "s"(thi), [tl4] "s"(tl4), [cap] "i"(SPIN_CAP)
        : "vcc", "memory");
    }
    {
      float a = om * xa, c = om * xb;
      a += dpp<ROR4>(a); c += dpp<ROR4>(c);
      a += dpp<ROR8>(a); c += dpp<ROR8>(c);
      const float o = sum32(tr16(a, c));
      auto sw = __builtin_amdgcn_permlane16_swap(__float_as_uint(o), __float_as_uint(o), false, false);
      const unsigned pq = cvtpk(__uint_as_float(sw[0]), __uint_as_float(sw[1]));
      const unsigned x2 = xr >> 1;
      const unsigned q0 = __builtin_amdgcn_readlane(pq, x2), q1 = __builtin_amdgcn_readlane(pq, 1u ^ x2), q2 = __builtin_amdgcn_readlane(pq, 2u ^ x2), q3 = __builtin_amdgcn_readlane(pq, 3u ^ x2);
      f32x4 f0, f1;
#pragma unroll
      for (int j = 0; j < 4; ++j) {
        const float lo = dot2(W[6][j], q3, dot2(W[4][j], q2, dot2(W[2][j], q1, dot2(W[0][j], q0, 0.f))));
        const float hi = dot2(W[7][j], q3, dot2(W[5][j], q2, dot2(W[3][j], q1, dot2(W[1][j], q0, 0.f))));
        if (j < 2) { f0[2 * j] = lo; f0[2 * j + 1] = hi; } else { f1[2 * j - 4] = lo; f1[2 * j - 3] = hi; }
      }
      *(f32x4*)(L_f + a_fw) = f0;
      *(f32x4*)(L_f + a_fw + 272) = f1;
    }
    __syncthreads();
    {
      const float* src = L_f + a_fr;
      const f32x2 u0 = *(const f32x2*)(src), u1 = *(const f32x2*)(src + 544), u2 = *(const f32x2*)(src + 2 * 544), u3 = *(const f32x2*)(src + 3 * 544);
      const f32x2 ts = (u0 + u1) + (u2 + u3);
      const v4u g = {g_lo(ts.x, tlo), thi, g_lo(ts.y, tlo), thi};
      ST128(g, rW, fpo, AUX_SC1);
    }
    SB();
    if (b < 64) {
      float gate = L_gate[0], omg = L_gate[1];
      PIN(gate); PIN(omg);
      __builtin_amdgcn_s_sleep(FDELAY);
      SB();
      float s0, s1;
      v4u u0, u1, u2, u3, u4, u5, u6, u7, u8, u9, u10, u11;
      {
        u64 ex, mi; unsigned sel, cnt, o1, o2, o3, tv, ta, tb;
#define F_SUM(S, X0, X1, X2, X3) \
        POLL_VAL("%[ta]", X0) POLL_VAL("%[tb]", X1) "v_add_f32_e32 " S ", %[ta], %[tb]\n\t" \
        POLL_VAL("%[ta]", X2) POLL_VAL("%[tb]", X3) "v_add_f32_e32 %[ta], %[ta], %[tb]\n\tv_add_f32_e32 " S ", " S ", %[ta]\n\t"
#define F_LD(A0, A3, B0, B3, C0, C3, D0, D3) \
        POLL_LD(A0, A3, "%[off]", "0", "") POLL_LD(B0, B3, "%[off]", "%[o1]", "") POLL_LD(C0, C3, "%[off]", "%[o2]", "") POLL_LD(D0, D3, "%[off]", "%[o3]", "")
#define F_ROUND(A0, A1, A2, A3, B0, B1, B2, B3, C0, C1, C2, C3, D0, D1, D2, D3) \
        "s_waitcnt vmcnt(8)\n\t" POLL_OK(A0, A1, A2, A3) POLL_OK(B0, B1, B2, B3) POLL_OK(C0, C1, C2, C3) POLL_OK(D0, D1, D2, D3) \
        F_SUM("%[s0]", A0, B0, C0, D0) F_SUM("%[s1]", A2, B2, C2, D2) POLL_NEXT F_LD(A0, A3, B0, B3, C0, C3, D0, D3)
        asm volatile(
          "s_mov_b64 %[ex], exec\n\ts_mov_b64 %[mi], exec\n\ts_mov_b32 %[sel], 0x04000400\n\ts_mov_b32 %[cnt], %[cap]\n\t"
          "s_mov_b32 %[o1], %[st1]\n\ts_mov_b32 %[o2], %[st2]\n\ts_mov_b32 %[o3], %[st3]\n\t"
          F_LD(56, 59, 60, 63, 64, 67, 68, 71)
          "s_sleep %[ps]\n\t"
          F_LD(72, 75, 76, 79, 80, 83, 84, 87)
          "s_sleep %[ps]\n\t"
          F_LD(88, 91, 92, 95, 96, 99, 100, 103)
          "1:\n\t"
          F_ROUND(56, 57, 58, 59, 60, 61, 62, 63, 64, 65, 66, 67, 68, 69, 70, 71)
          F_ROUND(72, 73, 74, 75, 76, 77, 78, 79, 80, 81, 82, 83, 84, 85, 86, 87)
          F_ROUND(88, 89, 90, 91, 92, 93, 94, 95, 96, 97, 98, 99, 100, 101, 102, 103)
          POLL_END
          : "=&{v[56:59]}"(u0), "=&{v[60:63]}"(u1), "=&{v[64:67]}"(u2), "=&{v[68:71]}"(u3), "=&{v[72:75]}"(u4), "=&{v[76:79]}"(u5),
            "=&{v[80:83]}"(u6), "=&{v[84:87]}"(u7), "=&{v[88:91]}"(u8), "=&{v[92:95]}"(u9), "=&{v[96:99]}"(u10), "=&{v[100:103]}"(u11),
            [s0] "=&v"(s0), [s1] "=&v"(s1), [tv] "=&v"(tv), [ta] "=&v"(ta), [tb] "=&v"(tb),
            [ex] "=&s"(ex), [mi] "=&s"(mi), [sel] "=&s"(sel), [cnt] "=&s"(cnt), [o1] "=&s"(o1), [o2] "=&s"(o2), [o3] "=&s"(o3)
          : [off] "v"(foff), [rs] "s"(rsW), [thi] "s"(thi), [tl4] "s"(tl4), [cap] "i"(SPIN_CAP), [ps] "i"(PSF),
            [st1] "i"(FL == 0 ? 0x10000 : 1024), [st2] "i"(FL == 0 ? 0x20000 : 2048), [st3] "i"(FL == 0 ? 0x30000 : 3072)
          : "vcc", "memory");
      }
      s0 += dpp<ROR4>(s0); s1 += dpp<ROR4>(s1);
      s0 += dpp<ROR8>(s0); s1 += dpp<ROR8>(s1);
      const float t = sum32(tr16(s0, s1));
      if (lane < 32 && (lane & 15) < 4) L_g[f * 8 + 2 * (lane & 3) + (lane >> 4)] = t;
      __syncthreads();
      if (f == 0) {
        const int c = lane & 7;
        const float a = L_g[c] + L_g[8 + c], bb = L_g[16 + c] + L_g[24 + c];
        const float r = rbf(gate * rbf(a)) + rbf(omg * rbf(bb));
        const float rn = dpp<XOR1>(r);
        const __amdgpu_buffer_rsrc_t rO = __builtin_amdgcn_make_buffer_rsrc((void*)p.out, 0, 1024, RS_FLAGS);
        __builtin_amdgcn_raw_buffer_store_b32(cvtpk(r, rn), rO, lane < 8 && !(lane & 1) ? b * 16 + lane * 2 : OOB, 0, 0);
        if (b == 0) __builtin_amdgcn_raw_buffer_store_b64(v2u{(unsigned)e64, (unsigned)(e64 >> 32)}, rW, lane == 0 ? WS_EPOCH : OOB, 0, AUX_SC1);
      }
      SB();
      asm volatile("" :: "{v[56:59]}"(u0), "{v[60:63]}"(u1), "{v[64:67]}"(u2), "{v[68:71]}"(u3), "{v[72:75]}"(u4), "{v[76:79]}"(u5),
                         "{v[80:83]}"(u6), "{v[84:87]}"(u7), "{v[88:91]}"(u8), "{v[92:95]}"(u9), "{v[96:99]}"(u10), "{v[100:103]}"(u11) : "memory");
    }
    asm volatile("" :: "{v[104:107]}"(t0), "{v[108:111]}"(t1), "{v[112:115]}"(t2) : "memory");
    asm volatile(".Lcode_e1:");
    return;
  }
  {
    const int f = wv;
    __builtin_amdgcn_s_setprio(3);
    const int qdst = WS_Q + ((br * 8 + ((kvh + XQ) & 7)) * 16 + s) * 512;
    const __amdgpu_buffer_rsrc_t rX = __builtin_amdgcn_make_buffer_rsrc((void*)p.x, 0, 1024, RS_FLAGS);
    const unsigned short* wqp = (br ? p.wqB : p.wqA) + (size_t)(s & 1) * 256 * 4096 + ((kvh + XQ) & 7) * 512 + (s >> 1) * 64;
    const __amdgpu_buffer_rsrc_t rQ = __builtin_amdgcn_make_buffer_rsrc((void*)wqp, 0, 255 * 8192 + 128, RS_FLAGS);
    unsigned pre = 0;
    if (f == 0) pre = __builtin_amdgcn_raw_buffer_load_b32(rW, lane < 4 ? qdst + lane * 128 : OOB, 0, AUX_SC1);
    v4u wq[8];
    {
      const int vo = (64 * f + 8 * (lane >> 3)) * 8192 + (lane & 7) * 16;
#pragma unroll
      for (int i = 0; i < 8; ++i) wq[i] = LD128(rQ, vo, i * 8192, AUX_NT);
    }
    const v4u xf = LD128(rX, ((s & 1) * 256 + 64 * f + 8 * (lane >> 3)) * 2, 0, 0);
    const v2u ep = __builtin_amdgcn_raw_buffer_load_b64(rW, WS_EPOCH, 0, AUX_SC1);
    v4u xa, ga;
    if (f == 3) {
      const __amdgpu_buffer_rsrc_t rG = __builtin_amdgcn_make_buffer_rsrc((void*)p.gw, 0, 1024, RS_FLAGS);
      xa = LD128(rX, lane * 16, 0, 0); ga = LD128(rG, lane * 16, 0, 0);
    }
    SB();
    if (tid < C_N) L_cnt[tid] = 0u;
    __syncthreads();
    SB();
    asm volatile("" :: "v"(cpt));
    const u64 e64 = (((u64)(unsigned)__builtin_amdgcn_readfirstlane(ep.y) << 32) | (unsigned)__builtin_amdgcn_readfirstlane(ep.x)) + 1ull;
    const unsigned tlo = (unsigned)e64 & 0xffu, thi = (unsigned)(e64 >> 8), tl4 = tlo * 0x01010101u;
    {
      float acc[8];
#pragma unroll
      for (int j = 0; j < 8; ++j) acc[j] = 0.f;
#pragma unroll
      for (int k = 0; k < 4; ++k)
#pragma unroll
        for (int j = 0; j < 4; ++j) {
          const unsigned lo = __builtin_amdgcn_perm(wq[2 * k + 1][j], wq[2 * k][j], 0x05040100u);
          const unsigned hi = __builtin_amdgcn_perm(wq[2 * k + 1][j], wq[2 * k][j], 0x07060302u);
          acc[2 * j] = dot2(lo, xf[k], acc[2 * j]);
          acc[2 * j + 1] = dot2(hi, xf[k], acc[2 * j + 1]);
        }
      float a4[4], a2[2];
#pragma unroll
      for (int j = 0; j < 4; ++j) a4[j] = tr32(acc[j], acc[j + 4]);
#pragma unroll
      for (int j = 0; j < 2; ++j) a2[j] = tr16(a4[j], a4[j + 2]);
      const float t0 = a2[0] + dpp<ROR8>(a2[0]), t1 = a2[1] + dpp<ROR8>(a2[1]);
      const float a1 = (lane & 8) ? t1 : t0;
      const int col = 8 * (lane & 7) + ((lane >> 3) & 1) + 2 * ((lane >> 4) & 1) + 4 * (lane >> 5);
      L_qred[f * 64 + col] = a1;
    }
    asm volatile("" :: "v"(pre));
    unsigned old;
    SIG_RTN(old, C_QP);
    if (old == 3u) {
      asm volatile("" ::: "memory");
      const int src = (2 * (lane >> 5)) * 64 + 2 * (lane & 31);
      const f32x2 t = *(const f32x2*)(L_qred + src) + *(const f32x2*)(L_qred + src + 64);
      const float q0 = sum32(t.x), q1 = sum32(t.y);
      const v4u g = {g_lo(q0, tlo), thi, g_lo(q1, tlo), thi};
      ST128(g, rW, lane < 32 ? qdst + lane * 16 : OOB, AUX_SC1);
      SIG(C_QS);
    }
    if (f == 3) {
      float gs = 0.f;
#pragma unroll
      for (int j = 0; j < 4; ++j) {
        const unsigned pr = cvtpk(bflo(xa[j]) * bflo(ga[j]), bfhi(xa[j]) * bfhi(ga[j]));
        gs += bflo(pr) + bfhi(pr);
      }
      gs += dpp<XOR1>(gs); gs += dpp<XOR2>(gs); gs += dpp<HMIR>(gs); gs += dpp<MIR>(gs);
      gs = sum16(gs); gs = sum32(gs);
      const float gate = rbf(__builtin_amdgcn_rcpf(1.f + __expf(-rbf(gs))));
      if (lane == 0) { L_gate[0] = gate; L_gate[1] = rbf(1.f - gate); }
    }
    const int qoff = WS_Q + (grp * 16 + 4 * f + 2 * (lane >> 5)) * 512 + (lane & 31) * 16;
    while (LDS_LD(L_cnt + C_QS) == 0u) __builtin_amdgcn_s_sleep(1);
    __builtin_amdgcn_s_sleep(QD1);
    SB();
    v4u t0, t1, t2, t3, t4, t5;
    {
      u64 ex, mi; unsigned sel, cnt, tv; float s0, s1, ta;
#define Q_LD(A0, A3, B0, B3) POLL_LD(A0, A3, "%[off]", "0", "") POLL_LD(B0, B3, "%[off]", "0", " offset:512")
#define Q_ROUND(W, A0, A1, A2, A3, B0, B1, B2, B3) \
      "s_waitcnt vmcnt(" W ")\n\t" POLL_OK(A0, A1, A2, A3) POLL_OK(B0, B1, B2, B3) \
      POLL_VAL("%[s0]", A0) POLL_VAL("%[ta]", B0) POLL_VAL("%[s1]", A2) "v_add_f32_e32 %[s0], %[s0], %[ta]\n\t" \
      POLL_VAL("%[ta]", B2) "v_add_f32_e32 %[s1], %[s1], %[ta]\n\t" POLL_NEXT Q_LD(A0, A3, B0, B3)
      asm volatile(
        "s_mov_b64 %[ex], exec\n\ts_mov_b64 %[mi], exec\n\ts_mov_b32 %[sel], 0x04000400\n\ts_mov_b32 %[cnt], %[cap]\n\t"
        Q_LD(104, 107, 108, 111)
        Q_ROUND("0", 104, 105, 106, 107, 108, 109, 110, 111)
        "s_sleep %[ps]\n\t"
        Q_LD(112, 115, 116, 119)
        "s_sleep %[ps]\n\t"
        Q_LD(120, 123, 124, 127)
        "1:\n\t"
        Q_ROUND("4", 104, 105, 106, 107, 108, 109, 110, 111)
        Q_ROUND("4", 112, 113, 114, 115, 116, 117, 118, 119)
        Q_ROUND("4", 120, 121, 122, 123, 124, 125, 126, 127)
        POLL_END
        : "=&{v[104:107]}"(t0), "=&{v[108:111]}"(t1), "=&{v[112:115]}"(t2), "=&{v[116:119]}"(t3), "=&{v[120:123]}"(t4), "=&{v[124:127]}"(t5),
          [s0] "=&v"(s0), [s1] "=&v"(s1), [ta] "=&v"(ta), [tv] "=&v"(tv),
          [ex] "=&s"(ex), [mi] "=&s"(mi), [sel] "=&s"(sel), [cnt] "=&s"(cnt)
        : [off] "v"(qoff), [rs] "s"(rsW), [thi] "s"(thi), [tl4] "s"(tl4), [cap] "i"(SPIN_CAP), [ps] "i"(PSQ)
        : "vcc", "memory");
      L_q[f * 64 + lane] = cvtpk(s0, s1);
    }
    SIG(C_QD);
    asm volatile("" :: "{v[104:107]}"(t0), "{v[108:111]}"(t1), "{v[112:115]}"(t2), "{v[116:119]}"(t3), "{v[120:123]}"(t4), "{v[124:127]}"(t5) : "memory");
    asm volatile(".Lcode_e0:");
  }
}

extern "C" void kernel_launch(void* const* d_in, const int* in_sizes, int n_in, void* d_out, int out_size, void* d_ws, size_t ws_size, hipStream_t stream) {
  Params p;
  p.x = (const unsigned short*)d_in[0]; p.kA = (const unsigned short*)d_in[1]; p.vA = (const unsigned short*)d_in[2];
  p.wqA = (const unsigned short*)d_in[3]; p.woA = (const unsigned short*)d_in[4];
  p.kB = (const unsigned short*)d_in[6]; p.vB = (const unsigned short*)d_in[7];
  p.wqB = (const unsigned short*)d_in[8]; p.woB = (const unsigned short*)d_in[9]; p.gw = (const unsigned short*)d_in[10];
  p.out = (unsigned short*)d_out; p.ws = (unsigned char*)d_ws + ws_off(ws_size);
  (void)hipFuncSetAttribute((const void*)fused_decode, hipFuncAttributeMaxDynamicSharedMemorySize, LDS_BYTES);
  fused_decode<<<256, 1024, LDS_BYTES, stream>>>(p);
}
```
